```python
import torch

def _softmax(x, axis=-1):
    return torch.softmax(x, dim=axis)
def _gelu(x):
    return torch.nn.functional.gelu(x, approximate="tanh")
def _silu(x):
    return torch.nn.functional.silu(x)
def _sigmoid(x):
    return torch.sigmoid(x)
def _layernorm(x, axis=-1, eps=1e-5):
    m = x.mean(dim=axis, keepdim=True)
    v = x.var(dim=axis, keepdim=True, unbiased=False)
    return (x - m) / torch.sqrt(v + eps)
def _scatter_add(tgt, idx, src, axis=0):
    return tgt.clone().index_add_(axis, idx, src)
def _segment_sum(src, seg, n):
    out = torch.zeros((n,) + src.shape[1:], dtype=src.dtype)
    return out.index_add_(0, seg, src)
def _pad(x, pw):
    flat = tuple(v for pair in reversed(pw) for v in pair)
    return torch.nn.functional.pad(x, flat)

kernel_name = 'parallel_sum__ssm_conv_s5__mixer_token_mlp__prompt'

def setup_inputs():

    _w = (lambda a: (lambda t: t.to(torch.bfloat16) if t.is_floating_point() else t)(torch.as_tensor(a)))
    def _A_setup():
        B, S, D = 1, 2048, 576
        P = D
        Lambda_re = -0.5 * (lambda _x: torch.exp(torch.as_tensor(_x)))((lambda *s: torch.randn(*s, dtype=torch.bfloat16) * 0.2)(P))
        Lambda_im = (lambda *s: torch.randn(*s, dtype=torch.bfloat16) * 0.8)(P) * 1.0
        B_re = (lambda *s: torch.randn(*s, dtype=torch.bfloat16) * 0.2)(P, D) * (D ** -0.5)
        B_im = (lambda *s: torch.randn(*s, dtype=torch.bfloat16) * 0.2)(P, D) * (D ** -0.5)
        C_re = (lambda *s: torch.randn(*s, dtype=torch.bfloat16) * 0.2)(D, P) * (P ** -0.5)
        C_im = (lambda *s: torch.randn(*s, dtype=torch.bfloat16) * 0.2)(D, P) * (P ** -0.5)
        log_dt = (lambda *s: torch.randn(*s, dtype=torch.bfloat16) * 0.2)(P) * 0.3 - 2.0
        Dskip = (lambda *s: torch.randn(*s, dtype=torch.bfloat16) * 0.05)(D)
        return {
            "x": _w((lambda *s: torch.randn(*s, dtype=torch.bfloat16) * 0.05)(B, S, D)),
            "Lambda_re": _w(Lambda_re),
            "Lambda_im": _w(Lambda_im),
            "B_re": _w(B_re),
            "B_im": _w(B_im),
            "C_re": _w(C_re),
            "C_im": _w(C_im),
            "log_dt": _w(log_dt),
            "Dskip": _w(Dskip),
        }
    _A_in = _A_setup()

    def _B_setup():
        B, S, D, F = 1, 2048, 576, 4608
        return {
            "x": _w((lambda *s: torch.randn(*s, dtype=torch.bfloat16) * 0.05)(B, S, D)),
            "w1": _w((lambda *s: torch.randn(*s, dtype=torch.bfloat16) * 0.8)(S, F) * (S ** -0.5)),
            "b1": _w((lambda *s: torch.randn(*s, dtype=torch.bfloat16) * 3.2)(F) * 0.02),
            "w2": _w((lambda *s: torch.randn(*s, dtype=torch.bfloat16) * 0.4)(F, S) * (F ** -0.5)),
            "b2": _w((lambda *s: torch.randn(*s, dtype=torch.bfloat16) * 3.2)(S) * 0.02),
        }
    _B_in = _B_setup()

    return {
        "A_x": _A_in["x"],
        "A_Lambda_re": _A_in["Lambda_re"],
        "A_Lambda_im": _A_in["Lambda_im"],
        "A_B_re": _A_in["B_re"],
        "A_B_im": _A_in["B_im"],
        "A_C_re": _A_in["C_re"],
        "A_C_im": _A_in["C_im"],
        "A_log_dt": _A_in["log_dt"],
        "A_Dskip": _A_in["Dskip"],
        "B_x": _B_in["x"],
        "B_w1": _B_in["w1"],
        "B_b1": _B_in["b1"],
        "B_w2": _B_in["w2"],
        "B_b2": _B_in["b2"],
    }

def reference(A_x, A_Lambda_re, A_Lambda_im, A_B_re, A_B_im, A_C_re, A_C_im, A_log_dt, A_Dskip, B_x, B_w1, B_b1, B_w2, B_b2):
    def _A_ref(x, Lambda_re, Lambda_im, B_re, B_im, C_re, C_im, log_dt, Dskip):
        Bsz, L, H = x.shape
        Lam = Lambda_re + 1j * Lambda_im
        Bmat = B_re + 1j * B_im
        Cmat = C_re + 1j * C_im
        dt = torch.exp(log_dt)
        Lam_bar = torch.exp(dt * Lam)
        B_bar = ((Lam_bar - 1.0) / Lam)[:, None] * Bmat
        Bu = torch.einsum("bld,pd->blp", x + 0j, B_bar)
        h = torch.zeros_like(Bu[:, 0, :])
        hs = []
        for t in range(L):
            h = Lam_bar[None, :] * h + Bu[:, t, :]
            hs.append(h)
        hseq = torch.stack(hs, 1)
        y = 2.0 * torch.einsum("blp,dp->bld", hseq, Cmat).real + x * Dskip[None, None, :]
        return y

    def _B_ref(x, w1, b1, w2, b2):
        B, S, D = x.shape
        h = _layernorm(x, axis=-1)
        u = torch.einsum("bsd,sf->bdf", h, w1) + b1
        u = _gelu(u)
        y = torch.einsum("bdf,fs->bds", u, w2) + b2
        return (lambda x, *d: x.permute(*d))(y, 0, 2, 1)

    x = A_x
    xn = _layernorm(x, axis=-1)
    return x + _A_ref(xn, A_Lambda_re, A_Lambda_im, A_B_re, A_B_im, A_C_re, A_C_im, A_log_dt, A_Dskip) + _B_ref(xn, B_w1, B_b1, B_w2, B_b2)
```

```cpp
#include <hip/hip_runtime.h>
#include <hip/hip_bf16.h>
typedef unsigned short u16;
__device__ __forceinline__ float bf2f(u16 b){ return __builtin_bit_cast(float, (unsigned)b<<16); }
__device__ __forceinline__ u16 f2bf(float f){ unsigned u=__builtin_bit_cast(unsigned,f); u += 0x7FFFu + ((u>>16)&1u); return (u16)(u>>16); }
__device__ __forceinline__ float rbf(float f){ return bf2f(f2bf(f)); }
constexpr int S=2048, D=576, P=576, F=4608;
__global__ void k_ln(const u16* x, u16* xn, u16* h){
  int s=blockIdx.x; int l=threadIdx.x; float v[9]; float sum=0;
  for(int i=0;i<9;i++){ v[i]=bf2f(x[s*D+l+64*i]); sum+=v[i]; }
  for(int pass=0;pass<2;pass++){
    sum=0; for(int i=0;i<9;i++) sum+=v[i];
    for(int o=32;o;o>>=1) sum+=__shfl_xor(sum,o);
    float m=sum/D; float q=0; for(int i=0;i<9;i++) q+=(v[i]-m)*(v[i]-m);
    for(int o=32;o;o>>=1) q+=__shfl_xor(q,o);
    float var=rbf(q/D); float mb=rbf(m); float den=rbf(sqrtf(rbf(var+1e-5f)));
    for(int i=0;i<9;i++){ v[i]=rbf(rbf(v[i]-mb)/den); (pass?h:xn)[s*D+l+64*i]=f2bf(v[i]); }
  }
}
__global__ void k_bu(const u16* xn, const u16* Bre, const u16* Bim, float* g){
  int p=blockIdx.x*64+threadIdx.x; int s=blockIdx.y; float a=0,b=0;
  for(int d=0;d<D;d++){ float xv=bf2f(xn[s*D+d]); a+=xv*bf2f(Bre[p*D+d]); b+=xv*bf2f(Bim[p*D+d]); }
  g[(s*P+p)*2]=a; g[(s*P+p)*2+1]=b;
}
__global__ void k_scan(float* g, const u16* lre, const u16* lim, const u16* ldt){
  int p=blockIdx.x*64+threadIdx.x; float dt=rbf(expf(bf2f(ldt[p]))); float lr=bf2f(lre[p]), li=bf2f(lim[p]);
  float e=expf(dt*lr); float br=e*cosf(dt*li), bi=e*sinf(dt*li);
  float nr=br-1.f, ni=bi; float den=lr*lr+li*li; float cr=(nr*lr+ni*li)/den, ci=(ni*lr-nr*li)/den;
  float hr=0,hi=0;
  for(int s=0;s<S;s++){ float gr=g[(s*P+p)*2], gi=g[(s*P+p)*2+1]; float ur=cr*gr-ci*gi, ui=cr*gi+ci*gr;
    float tr=br*hr-bi*hi+ur, ti=br*hi+bi*hr+ui; hr=tr; hi=ti; g[(s*P+p)*2]=hr; g[(s*P+p)*2+1]=hi; }
}
__global__ void k_g1(const u16* h, const u16* w1, const u16* b1, u16* u){
  int f=blockIdx.x*64+threadIdx.x; int d=blockIdx.y; float a=0;
  for(int s=0;s<S;s++) a+=bf2f(h[s*D+d])*bf2f(w1[s*F+f]);
  float t=rbf(rbf(a)+bf2f(b1[f])); float gl=0.5f*t*(1.f+tanhf(0.7978845608028654f*(t+0.044715f*t*t*t)));
  u[d*F+f]=f2bf(gl);
}
__global__ void k_g2(const u16* u, const u16* w2, const u16* b2, const float* hs, const u16* Cre, const u16* Cim, const u16* x, const u16* xn, const u16* dsk, float* out){
  int s=blockIdx.x*64+threadIdx.x; int d=blockIdx.y; float a=0;
  for(int f=0;f<F;f++) a+=bf2f(u[d*F+f])*bf2f(w2[f*S+s]);
  float yb=rbf(rbf(a)+bf2f(b2[s])); float c=0;
  for(int p=0;p<P;p++) c+=hs[(s*P+p)*2]*bf2f(Cre[d*P+p])-hs[(s*P+p)*2+1]*bf2f(Cim[d*P+p]);
  float ya=2.f*c+rbf(bf2f(xn[s*D+d])*bf2f(dsk[d]));
  out[s*D+d]=bf2f(x[s*D+d])+ya+yb;
}
extern "C" void kernel_launch(void* const* d_in, const int* in_sizes, int n_in, void* d_out, int out_size, void* d_ws, size_t ws_size, hipStream_t stream) {
  const u16* x=(const u16*)d_in[0]; const u16* lre=(const u16*)d_in[1]; const u16* lim=(const u16*)d_in[2];
  const u16* Bre=(const u16*)d_in[3]; const u16* Bim=(const u16*)d_in[4]; const u16* Cre=(const u16*)d_in[5]; const u16* Cim=(const u16*)d_in[6];
  const u16* ldt=(const u16*)d_in[7]; const u16* dsk=(const u16*)d_in[8]; const u16* w1=(const u16*)d_in[10]; const u16* b1=(const u16*)d_in[11];
  const u16* w2=(const u16*)d_in[12]; const u16* b2=(const u16*)d_in[13];
  char* ws=(char*)d_ws; u16* xn=(u16*)ws; u16* h=(u16*)(ws+(4<<20)); float* g=(float*)(ws+(8<<20)); u16* u=(u16*)(ws+(32<<20));
  k_ln<<<S,64,0,stream>>>(x,xn,h);
  k_bu<<<dim3(P/64,S),64,0,stream>>>(xn,Bre,Bim,g);
  k_scan<<<P/64,64,0,stream>>>(g,lre,lim,ldt);
  k_g1<<<dim3(F/64,D),64,0,stream>>>(h,w1,b1,u);
  k_g2<<<dim3(S/64,D),64,0,stream>>>(u,w2,b2,g,Cre,Cim,x,xn,dsk,(float*)d_out);
}
```

```cpp
#include <hip/hip_runtime.h>
#include <hip/hip_bf16.h>
typedef unsigned short u16;
__device__ __forceinline__ float bf2f(u16 b){ return __builtin_bit_cast(float, (unsigned)b<<16); }
__device__ __forceinline__ u16 f2bf(float f){ unsigned u=__builtin_bit_cast(unsigned,f); u += 0x7FFFu + ((u>>16)&1u); return (u16)(u>>16); }
__device__ __forceinline__ float rbf(float f){ return bf2f(f2bf(f)); }
constexpr int S=2048, D=576, P=576, F=4608;
__global__ void k_ln(const u16* x, u16* xn, u16* h){
  int s=blockIdx.x; int l=threadIdx.x; float v[9]; float sum=0;
  for(int i=0;i<9;i++){ v[i]=bf2f(x[s*D+l+64*i]); sum+=v[i]; }
  for(int pass=0;pass<2;pass++){
    sum=0; for(int i=0;i<9;i++) sum+=v[i];
    for(int o=32;o;o>>=1) sum+=__shfl_xor(sum,o);
    float m=sum/D; float q=0; for(int i=0;i<9;i++) q+=(v[i]-m)*(v[i]-m);
    for(int o=32;o;o>>=1) q+=__shfl_xor(q,o);
    float var=rbf(q/D); float mb=rbf(m); float den=rbf(sqrtf(rbf(var+1e-5f)));
    for(int i=0;i<9;i++){ v[i]=rbf(rbf(v[i]-mb)/den); (pass?h:xn)[s*D+l+64*i]=f2bf(v[i]); }
  }
}
__global__ void k_bu(const u16* xn, const u16* Bre, const u16* Bim, float* g){
  int p=blockIdx.x*64+threadIdx.x; int s=blockIdx.y; float a=0,b=0;
  for(int d=0;d<D;d++){ float xv=bf2f(xn[s*D+d]); a+=xv*bf2f(Bre[p*D+d]); b+=xv*bf2f(Bim[p*D+d]); }
  g[(s*P+p)*2]=a; g[(s*P+p)*2+1]=b;
}
__global__ void k_scan(float* g, const u16* lre, const u16* lim, const u16* ldt){
  int p=blockIdx.x*64+threadIdx.x; float dt=rbf(expf(bf2f(ldt[p]))); float lr=bf2f(lre[p]), li=bf2f(lim[p]);
  float e=expf(dt*lr); float br=e*cosf(dt*li), bi=e*sinf(dt*li);
  float nr=br-1.f, ni=bi; float den=lr*lr+li*li; float cr=(nr*lr+ni*li)/den, ci=(ni*lr-nr*li)/den;
  float hr=0,hi=0;
  for(int s=0;s<S;s++){ float gr=g[(s*P+p)*2], gi=g[(s*P+p)*2+1]; float ur=cr*gr-ci*gi, ui=cr*gi+ci*gr;
    float tr=br*hr-bi*hi+ur, ti=br*hi+bi*hr+ui; hr=tr; hi=ti; g[(s*P+p)*2]=hr; g[(s*P+p)*2+1]=hi; }
}
__global__ void k_g1(const u16* h, const u16* w1, const u16* b1, u16* u){
  int f=blockIdx.x*64+threadIdx.x; int d=blockIdx.y; float a=0;
  for(int s=0;s<S;s++) a+=bf2f(h[s*D+d])*bf2f(w1[s*F+f]);
  float t=rbf(rbf(a)+bf2f(b1[f])); float gl=0.5f*t*(1.f+tanhf(0.7978845608028654f*(t+0.044715f*t*t*t)));
  u[d*F+f]=f2bf(gl);
}
__global__ void k_g2(const u16* u, const u16* w2, const u16* b2, const float* hs, const u16* Cre, const u16* Cim, const u16* x, const u16* xn, const u16* dsk, float* out){
  int s=blockIdx.x*64+threadIdx.x; int d=blockIdx.y; float a=0;
  for(int f=0;f<F;f++) a+=bf2f(u[d*F+f])*bf2f(w2[f*S+s]);
  float yb=rbf(rbf(a)+bf2f(b2[s])); float c=0;
  for(int p=0;p<P;p++) c+=hs[(s*P+p)*2]*bf2f(Cre[d*P+p])-hs[(s*P+p)*2+1]*bf2f(Cim[d*P+p]);
  float ya=2.f*c+rbf(bf2f(xn[s*D+d])*bf2f(dsk[d]));
  out[s*D+d]=bf2f(x[s*D+d])+ya+yb;
}

typedef unsigned v4u __attribute__((ext_vector_type(4)));
template<int AUX> __global__ __launch_bounds__(256) void k_rd(const char* a, const char* b, v4u* o){
  __amdgpu_buffer_rsrc_t ra=__builtin_amdgcn_make_buffer_rsrc((void*)a,0,S*F*2,0x00020000);
  __amdgpu_buffer_rsrc_t rb=__builtin_amdgcn_make_buffer_rsrc((void*)b,0,S*F*2,0x00020000);
  v4u x={0,0,0,0}; int t=blockIdx.x*256+threadIdx.x;
  #pragma unroll
  for(int j=0;j<9;j++){ x^=__builtin_amdgcn_raw_buffer_load_b128(ra,(j*131072+t)*16,0,AUX); x^=__builtin_amdgcn_raw_buffer_load_b128(rb,(j*131072+t)*16,0,AUX);}
  if(x[0]==0x12345678 && x[1]==0x9abcdef0 && x[2]==77) o[0]=x;
}
template<int AUX> __global__ __launch_bounds__(256) void k_wr(char* p){
  __amdgpu_buffer_rsrc_t r=__builtin_amdgcn_make_buffer_rsrc((void*)p,0,32<<20,0x00020000);
  int t=blockIdx.x*256+threadIdx.x; v4u x={1u,2u,3u,(unsigned)t};
  #pragma unroll
  for(int j=0;j<16;j++) __builtin_amdgcn_raw_buffer_store_b128(x,r,(j*131072+t)*16,0,AUX);
}
extern "C" void kernel_launch(void* const* d_in, const int* in_sizes, int n_in, void* d_out, int out_size, void* d_ws, size_t ws_size, hipStream_t stream) {
  const u16* x=(const u16*)d_in[0]; const u16* lre=(const u16*)d_in[1]; const u16* lim=(const u16*)d_in[2];
  const u16* Bre=(const u16*)d_in[3]; const u16* Bim=(const u16*)d_in[4]; const u16* Cre=(const u16*)d_in[5]; const u16* Cim=(const u16*)d_in[6];
  const u16* ldt=(const u16*)d_in[7]; const u16* dsk=(const u16*)d_in[8]; const u16* w1=(const u16*)d_in[10]; const u16* b1=(const u16*)d_in[11];
  const u16* w2=(const u16*)d_in[12]; const u16* b2=(const u16*)d_in[13];
  char* ws=(char*)d_ws; u16* xn=(u16*)ws; u16* h=(u16*)(ws+(4<<20)); float* g=(float*)(ws+(8<<20)); u16* u=(u16*)(ws+(32<<20));
  k_rd<0><<<512,256,0,stream>>>((const char*)w1,(const char*)w2,(v4u*)(ws+(60<<20)));
  k_ln<<<S,64,0,stream>>>(x,xn,h);
  k_bu<<<dim3(P/64,S),64,0,stream>>>(xn,Bre,Bim,g);
  k_scan<<<P/64,64,0,stream>>>(g,lre,lim,ldt);
  k_g1<<<dim3(F/64,D),64,0,stream>>>(h,w1,b1,u);
  k_g2<<<dim3(S/64,D),64,0,stream>>>(u,w2,b2,g,Cre,Cim,x,xn,dsk,(float*)d_out);
}
```

```cpp
#include <hip/hip_runtime.h>

typedef unsigned short u16;
typedef unsigned long long u64;
typedef unsigned v4u __attribute__((ext_vector_type(4)));
typedef unsigned v2u __attribute__((ext_vector_type(2)));
typedef float f32x4 __attribute__((ext_vector_type(4)));
typedef float f32x2 __attribute__((ext_vector_type(2)));
typedef short s16x8 __attribute__((ext_vector_type(8)));
typedef short s16x4 __attribute__((ext_vector_type(4)));
typedef __bf16 bf16x2 __attribute__((ext_vector_type(2)));
typedef __amdgpu_buffer_rsrc_t rsrc_t;
#define LAS __attribute__((address_space(3)))
#define DEVI __device__ __forceinline__

#define TS 0
#define POLL_DELAY_H 8
#define POLL_DELAY_U 8
#define LEAD1 7
#define LEAD2 5

constexpr int S = 2048, D = 576, F = 4608;
constexpr unsigned WS_EPOCH = 0, WS_FH = 4096, WS_FU = 12288, WS_FT = 20480, WS_FX = 24576, WS_AGG = 65536,
                   WS_STAT = 147456, WS_HF = 1u << 20, WS_UF = 4u << 20, WS_HT = 10u << 20, WS_XS = 16u << 20,
                   WS_TS = 32u << 20, WS_BYTES = 40u << 20;
constexpr unsigned L_F = 98304, L_MISC = 153600, L_TOTAL = 163840;
constexpr unsigned L_AFLG = 131072, L_ASTAT = 135168;
constexpr int SPIN_CAP = 1 << 15;

DEVI float asf(unsigned u) { return __builtin_bit_cast(float, u); }
DEVI unsigned asu(float f) { return __builtin_bit_cast(unsigned, f); }
DEVI bf16x2 asb(unsigned u) { return __builtin_bit_cast(bf16x2, u); }
DEVI float bflo(unsigned u) { return __builtin_bit_cast(float, u << 16); }
DEVI float bfhi(unsigned u) { return __builtin_bit_cast(float, u & 0xffff0000u); }
DEVI unsigned pk2(float a, float b) {
  f32x2 v = {a, b};
  return __builtin_bit_cast(unsigned, __builtin_convertvector(v, bf16x2));
}
DEVI float rbf(float a) { return bflo(pk2(a, a)); }
DEVI f32x4 mfma(v4u a, v4u b, f32x4 c) {
  return __builtin_amdgcn_mfma_f32_16x16x32_bf16(__builtin_bit_cast(s16x8, a), __builtin_bit_cast(s16x8, b), c, 0, 0, 0);
}
DEVI rsrc_t mk(const void* p, unsigned bytes) { return __builtin_amdgcn_make_buffer_rsrc((void*)p, 0, bytes, 0x00020000); }
template <int CTRL> DEVI float dadd(float s) {
  return s + __builtin_bit_cast(float, __builtin_amdgcn_update_dpp(0, __builtin_bit_cast(int, s), CTRL, 0xf, 0xf, true));
}
DEVI float x16(float s) {
  unsigned u = __builtin_bit_cast(unsigned, s);
  auto r = __builtin_amdgcn_permlane16_swap(u, u, false, false);
  return asf(r[0]) + asf(r[1]);
}
DEVI float x32(float s) {
  unsigned u = __builtin_bit_cast(unsigned, s);
  auto r = __builtin_amdgcn_permlane32_swap(u, u, false, false);
  return asf(r[0]) + asf(r[1]);
}
DEVI float red32(float s) {
  s = dadd<0xB1>(s); s = dadd<0x4E>(s); s = dadd<0x141>(s); s = dadd<0x140>(s);
  return x16(s);
}
#define SB() __builtin_amdgcn_sched_barrier(0)
#define BAR() do { SB(); asm volatile("s_waitcnt lgkmcnt(0)\n\ts_barrier" ::: "memory"); SB(); } while (0)
#define WAITV(n) asm volatile("s_waitcnt vmcnt(" #n ")" ::: "memory")
template <int POL, int OFF> DEVI void dma(unsigned lds, unsigned voff, rsrc_t r, unsigned soff) {
  if constexpr (POL == 0)
    asm volatile("s_mov_b32 m0, %0\n\ts_nop 0\n\tbuffer_load_dwordx4 %1, %2, %3 offen offset:%4 lds" ::"s"(lds), "v"(voff), "s"(r), "s"(soff), "i"(OFF) : "memory");
  else if constexpr (POL == 1)
    asm volatile("s_mov_b32 m0, %0\n\ts_nop 0\n\tbuffer_load_dwordx4 %1, %2, %3 offen offset:%4 nt lds" ::"s"(lds), "v"(voff), "s"(r), "s"(soff), "i"(OFF) : "memory");
  else
    asm volatile("s_mov_b32 m0, %0\n\ts_nop 0\n\tbuffer_load_dwordx4 %1, %2, %3 offen offset:%4 sc1 lds" ::"s"(lds), "v"(voff), "s"(r), "s"(soff), "i"(OFF) : "memory");
}
DEVI v4u lds128(unsigned a) { return *(LAS v4u*)(size_t)a; }
DEVI v2u ldstr(unsigned a) { return __builtin_bit_cast(v2u, __builtin_amdgcn_ds_read_tr16_b64_v4i16((LAS s16x4*)(size_t)a)); }
DEVI int rl(int v, int i) { return __builtin_amdgcn_readlane(v, i); }
DEVI int order_table(int key, int n, int lane) {
  key = lane < n ? key * 64 + lane : 0x40000000 + lane;
  int rank = 0;
  _Pragma("unroll 1") for (int m = 0; m < n; m++) rank += rl(key, m) < key ? 1 : 0;
  if (lane >= n) rank = lane;
  return __builtin_amdgcn_ds_permute(rank * 4, lane);
}
template <int NL> DEVI void poll(rsrc_t rs, unsigned off, unsigned step, bool valid, unsigned tlo, unsigned thi) {
  _Pragma("unroll 1") for (int it = 0; it < SPIN_CAP; it++) {
    asm volatile("" ::: "memory");
    bool ok = true;
#pragma unroll
    for (int j = 0; j < NL; j++) {
      v2u a = __builtin_amdgcn_raw_buffer_load_b64(rs, off + j * step, 0, 16);
      ok = ok && a[0] == tlo && a[1] == thi;
    }
    if (__all(ok || !valid)) break;
    __builtin_amdgcn_s_sleep(4);
  }
  asm volatile("" ::: "memory");
}
DEVI void put_flag(rsrc_t rs, unsigned off, unsigned tlo, unsigned thi) {
  v2u t = {tlo, thi};
  __builtin_amdgcn_raw_buffer_store_b64(t, rs, off, 0, 16);
}
#define STAMP(i) do {} while (0)

__global__ __launch_bounds__(512, 2) void fused_kernel(
    const u16* __restrict__ x, const u16* __restrict__ lre, const u16* __restrict__ lim, const u16* __restrict__ Bre,
    const u16* __restrict__ Bim, const u16* __restrict__ Cre, const u16* __restrict__ Cim, const u16* __restrict__ ldt,
    const u16* __restrict__ dsk, const u16* __restrict__ w1, const u16* __restrict__ b1, const u16* __restrict__ w2,
    const u16* __restrict__ b2, float* __restrict__ out, char* __restrict__ ws) {
  extern __shared__ __attribute__((aligned(16))) char smem[];
  const int b = blockIdx.x, xcd = b & 7, idx = b >> 3, q = xcd >> 1, odd = xcd & 1;
  const int lane = threadIdx.x & 63, w = __builtin_amdgcn_readfirstlane(threadIdx.x >> 6);
  const int g = lane >> 4, n = lane & 15;
  const int ng1 = odd ? 24 : 30;
  const bool isA = idx >= ng1;
  const rsrc_t rs = mk(ws, WS_BYTES);
  const rsrc_t rx = mk(x, S * D * 2);
  if (w == 0) STAMP(0);
  const int idx2 = (idx + 8 * q) & 31, st = odd * 8 + (idx2 >> 2), j2 = idx2 & 3, tile = odd * 32 + idx2;
  const int qR = ((idx2 >> 3) + 1) & 3;
  const bool hasR = odd || (idx2 & 7) >= 6;
  const int nss = hasR ? (isA ? 0 : 6) : (q < 2 ? 5 : 4);
  const int ss0 = hasR ? 6 * (q - (q > qR ? 1 : 0)) : (q < 2 ? 5 * q : 4 * q + 2);
  const int n2 = 18 + nss;
  const unsigned rowsrc = (lane >> 3) * 1152 + (((lane & 7) ^ (lane >> 3)) << 4);
  const unsigned rowrd0 = n * 128 + ((g ^ (n & 7)) << 4), rowrd1 = n * 128 + (((4 + g) ^ (n & 7)) << 4);
  const rsrc_t rw2 = mk(w2, F * S * 2);
  const int wl = w & 1;
  unsigned vw2[4];
#pragma unroll
  for (int jj = 0; jj < 4; jj++) vw2[jj] = g * 4096 + st * 256 + ((n ^ ((g << 2) | jj)) << 4);
  int ord2 = 0;
  if (w >= 4) ord2 = order_table(lane - ((lane & 3) == j2 ? LEAD2 : 0), 18, lane);
  auto w2_issue = [&](int k) {
    const int sg = rl(ord2, k);
    const unsigned lds = ((k + 2) % 6) * 16384 + wl * 8192, so = ((18 * q + sg) * 64 + 32 * wl) * 4096;
    if ((sg & 3) == j2) {
#pragma unroll
      for (int j = 0; j < 8; j++) dma<1, 0>(lds + j * 1024, vw2[j & 3], rw2, so + (16 * (j & 1) + 4 * (j >> 1)) * 4096);
    } else {
#pragma unroll
      for (int j = 0; j < 8; j++) dma<0, 0>(lds + j * 1024, vw2[j & 3], rw2, so + (16 * (j & 1) + 4 * (j >> 1)) * 4096);
    }
  };
  unsigned tlo, thi;

  if (!isA) {
    const int ft = 9 * q + (odd ? 5 : 0) + idx / 6, dt = idx % 6;
    const int gi = (odd ? 30 : 0) + idx;
    const int grp = w >> 2, w4 = w & 3;
    const bool lnact = grp == 0 || gi < 10;
    const int unit = q * 64 + (grp ? 54 + gi : gi);
    unsigned xv[9];
    const int r2 = lane >> 5, c = lane & 31;
    if (lnact) {
#pragma unroll
      for (int j = 0; j < 9; j++)
        xv[j] = __builtin_amdgcn_raw_buffer_load_b32(rx, (unit * 8 + w4 * 2 + r2) * 1152 + (j * 32 + c) * 4, 0, 0);
    }
    if (w == 0) *(LAS v2u*)(size_t)(L_MISC + 2048) = __builtin_amdgcn_raw_buffer_load_b64(rs, WS_EPOCH, 0, 16);
    int ord1 = 0;
    if (w >= 4) ord1 = order_table(lane - ((lane % 6) == dt ? LEAD1 : 0), 32, lane);
    const rsrc_t rw1 = mk(w1, S * F * 2);
    unsigned vw1[4];
#pragma unroll
    for (int jj = 0; jj < 4; jj++) vw1[jj] = g * 9216 + ft * 256 + ((n ^ ((g << 2) | jj)) << 4);
    auto w1_issue = [&](int i) {
      const int sg = rl(ord1, i);
      const unsigned lds = (i % 6) * 16384 + wl * 8192, so = (sg * 64 + 32 * wl) * 9216;
      if (sg % 6 == dt) {
#pragma unroll
        for (int j = 0; j < 8; j++) dma<1, 0>(lds + j * 1024, vw1[j & 3], rw1, so + 4 * j * 9216);
      } else {
#pragma unroll
        for (int j = 0; j < 8; j++) dma<0, 0>(lds + j * 1024, vw1[j & 3], rw1, so + 4 * j * 9216);
      }
    };
    if (w == 4 || w == 5) { _Pragma("unroll 1") for (int i = 0; i < 2; i++) w1_issue(i); }
    if (lnact) {
      float v[18];
#pragma unroll
      for (int j = 0; j < 9; j++) { v[2 * j] = bflo(xv[j]); v[2 * j + 1] = bfhi(xv[j]); }
      float s = 0.f;
#pragma unroll
      for (int j = 0; j < 18; j++) s += v[j];
      const float mean = red32(s) * (1.f / D);
      float qq = 0.f;
#pragma unroll
      for (int j = 0; j < 18; j++) qq += (v[j] - mean) * (v[j] - mean);
      const float var = red32(qq) * (1.f / D);
      const float mb = rbf(mean), rs1 = 1.f / rbf(sqrtf(rbf(rbf(var) + 1e-5f)));
      float s2 = 0.f, q2 = 0.f;
#pragma unroll
      for (int j = 0; j < 9; j++) {
        unsigned u = pk2(v[2 * j] - mb, v[2 * j + 1] - mb);
        u = pk2(bflo(u) * rs1, bfhi(u) * rs1);
        v[2 * j] = bflo(u); v[2 * j + 1] = bfhi(u);
        s2 += v[2 * j] + v[2 * j + 1];
        q2 += v[2 * j] * v[2 * j] + v[2 * j + 1] * v[2 * j + 1];
      }
      const float mean2 = red32(s2) * (1.f / D);
      const float var2 = fmaxf(red32(q2) * (1.f / D) - mean2 * mean2, 0.f);
      const float mb2 = rbf(mean2), rs2 = 1.f / rbf(sqrtf(rbf(rbf(var2) + 1e-5f)));
      const unsigned sel = r2 ? 0x07060302u : 0x05040100u;
      const unsigned la = L_F + grp * 9216 + w4 * 2304 + (2 * c + r2) * 4;
#pragma unroll
      for (int j = 0; j < 9; j++) {
        unsigned u = pk2(v[2 * j] - mb2, v[2 * j + 1] - mb2);
        u = pk2(bflo(u) * rs2, bfhi(u) * rs2);
        auto sw = __builtin_amdgcn_permlane32_swap(u, u, false, false);
        *(LAS unsigned*)(size_t)(la + j * 256) = __builtin_amdgcn_perm(sw[1], sw[0], sel);
      }
      if (c == 0) {
        v2u sv = {__builtin_bit_cast(unsigned, mb), __builtin_bit_cast(unsigned, rs1)};
        __builtin_amdgcn_raw_buffer_store_b64(sv, rs, WS_STAT + (unit * 8 + w4 * 2 + r2) * 8, 0, 16);
      }
    }
    BAR();
    {
      const v2u e = *(LAS v2u*)(size_t)(L_MISC + 2048);
      const u64 t = (((u64)e[1] << 32) | e[0]) + 1;
      tlo = __builtin_amdgcn_readfirstlane((unsigned)t);
      thi = __builtin_amdgcn_readfirstlane((unsigned)(t >> 32));
    }
    if (lnact) {
      _Pragma("unroll 1") for (int k = w4; k < 9; k += 4) {
        const int d = 64 * k + lane;
        const unsigned la = L_F + grp * 9216 + d * 4;
        v4u o;
#pragma unroll
        for (int j = 0; j < 4; j++) o[j] = *(LAS unsigned*)(size_t)(la + j * 2304);
        const int d96 = d / 96, nt = (d - d96 * 96) >> 4;
        __builtin_amdgcn_raw_buffer_store_b128(o, rs, d96 * 393216 + nt * 2048 + n * 16,
                                               WS_HF + (unit >> 3) * 12288 + ((unit >> 2) & 1) * 1024 + (unit & 3) * 256, 16);
      }
      WAITV(0);
    }
    BAR();
    if (lnact && w4 == 0 && lane == 0) put_flag(rs, WS_FH + unit * 32, tlo, thi);
    if (w == 0) STAMP(1);

    if (w < 4) {
      const rsrc_t rb1 = mk(b1, F * 2);
      v2u bia[2];
#pragma unroll
      for (int cc = 0; cc < 2; cc++) bia[cc] = __builtin_amdgcn_raw_buffer_load_b64(rb1, (ft * 128 + 32 * w + 16 * cc + 4 * g) * 2, 0, 0);
      unsigned ta[2][2];
#pragma unroll
      for (int cc = 0; cc < 2; cc++)
#pragma unroll
        for (int t = 0; t < 2; t++)
          ta[cc][t] = 256 * (8 * g + 4 * t + (n >> 2)) + (((4 * w + 2 * cc + ((n & 3) >> 1)) ^ (((n >> 2) << 2) | ((2 * g + t) & 3))) << 4) + 8 * (n & 1);
      f32x4 acc[2][6];
#pragma unroll
      for (int cc = 0; cc < 2; cc++)
#pragma unroll
        for (int nt = 0; nt < 6; nt++) acc[cc][nt] = f32x4{0.f, 0.f, 0.f, 0.f};
      BAR();
      unsigned wo = 0;
      _Pragma("unroll 1") for (int i = 0; i < 32; i++) {
        BAR();
        if (i == 0 && w == 0) STAMP(3);
        const unsigned fo = L_F + (i & 3) * 12288 + lane * 16;
        v4u a[2][2], bb[2][6];
#pragma unroll
        for (int ks = 0; ks < 2; ks++) {
#pragma unroll
          for (int cc = 0; cc < 2; cc++) {
            v2u lo = ldstr(ta[cc][0] + wo + ks * 8192), hi = ldstr(ta[cc][1] + wo + ks * 8192);
            a[ks][cc] = v4u{lo[0], lo[1], hi[0], hi[1]};
          }
#pragma unroll
          for (int nt = 0; nt < 6; nt++) bb[ks][nt] = lds128(fo + nt * 2048 + ks * 1024);
          SB();
        }
#pragma unroll
        for (int ks = 0; ks < 2; ks++)
#pragma unroll
          for (int nt = 0; nt < 6; nt++)
#pragma unroll
            for (int cc = 0; cc < 2; cc++) acc[cc][nt] = mfma(a[ks][cc], bb[ks][nt], acc[cc][nt]);
        wo = wo == 5 * 16384 ? 0 : wo + 16384;
      }
      BAR();
      if (w == 0) STAMP(4);
      float bf[2][4];
#pragma unroll
      for (int cc = 0; cc < 2; cc++) { bf[cc][0] = bflo(bia[cc][0]); bf[cc][1] = bfhi(bia[cc][0]); bf[cc][2] = bflo(bia[cc][1]); bf[cc][3] = bfhi(bia[cc][1]); }
#pragma unroll
      for (int nt = 0; nt < 6; nt++) {
        v4u o;
#pragma unroll
        for (int cc = 0; cc < 2; cc++)
#pragma unroll
          for (int h = 0; h < 2; h++) {
            unsigned u = pk2(acc[cc][nt][2 * h], acc[cc][nt][2 * h + 1]);
            u = pk2(bflo(u) + bf[cc][2 * h], bfhi(u) + bf[cc][2 * h + 1]);
            float y[2] = {bflo(u), bfhi(u)};
#pragma unroll
            for (int e = 0; e < 2; e++) {
              const float t = y[e];
              const float z = t * (-2.302208198f + -0.1029432397f * t * t);
              y[e] = t * __builtin_amdgcn_rcpf(1.f + __builtin_amdgcn_exp2f(z));
            }
            o[2 * cc + h] = pk2(y[0], y[1]);
          }
        const int d16 = 6 * dt + nt, jj = d16 / 9, nt2 = d16 - 9 * jj;
        __builtin_amdgcn_raw_buffer_store_b128(o, rs, lane * 16,
                                               WS_UF + jj * (72 * 18432) + (2 * ft + (w >> 1)) * 18432 + nt2 * 2048 + (w & 1) * 1024, 16);
      }
      WAITV(0);
      BAR();
      if (w == 0 && lane == 0) put_flag(rs, WS_FU + (ft * 6 + dt) * 32, tlo, thi);
      if (w == 0) STAMP(5);
    } else if (w < 6) {
      _Pragma("unroll 1") for (int i = 2; i < 6; i++) w1_issue(i);
      BAR();
      WAITV(40);
      BAR();
      _Pragma("unroll 1") for (int i = 1; i < 32; i++) {
        WAITV(32);
        BAR();
        if (i + 5 < 32) w1_issue(i + 5); else w2_issue(i + 5 - 32);
      }
      BAR();
      w2_issue(5);
      BAR();
    } else {
      __builtin_amdgcn_s_sleep(POLL_DELAY_H);
      poll<2>(rs, WS_FH + (wl * 128 + lane) * 32, 64 * 32, true, tlo, thi);
      if (w == 6) STAMP(2);
      BAR();
      auto f1_issue = [&](int i) {
        const int sg = rl(ord1, i);
        const unsigned lds = L_F + (i & 3) * 12288 + wl * 6144, so = WS_HF + dt * 393216 + sg * 12288 + wl * 6144;
        dma<2, 0>(lds, lane * 16, rs, so); dma<2, 1024>(lds, lane * 16, rs, so);
        dma<2, 2048>(lds, lane * 16, rs, so); dma<2, 3072>(lds, lane * 16, rs, so);
        dma<2, 0>(lds + 4096, lane * 16, rs, so + 4096); dma<2, 1024>(lds + 4096, lane * 16, rs, so + 4096);
      };
      _Pragma("unroll 1") for (int i = 0; i < 4; i++) f1_issue(i);
      WAITV(18);
      BAR();
      _Pragma("unroll 1") for (int i = 1; i < 30; i++) {
        WAITV(12);
        BAR();
        if (i + 3 < 32) f1_issue(i + 3);
      }
      WAITV(6);
      BAR();
      WAITV(0);
      BAR();
      BAR();
      BAR();
    }
  } else {
    const int a = odd ? idx - 22 : idx - 30;
    const int chunk = 2 * q + (a >= 5 ? 1 : 0), pg = a >= 5 ? a - 5 : a;
    const int t0 = pg == 0 ? 0 : 7 * pg + 1;
    const bool act = w < 7 || pg == 0;
    const int p0 = (t0 + w) * 16;
    const rsrc_t rbr = mk(Bre, D * D * 2), rbi = mk(Bim, D * D * 2);
    const unsigned vx = (64 * (lane >> 5) + ((lane >> 3) & 3)) * 1152 + (((lane & 7) ^ (lane >> 3)) << 4);
    auto a_issue = [&](int i) {
      const unsigned lds = (i & 1) * 65536;
#pragma unroll
      for (int j = 0; j < 4; j++) {
        const int u = 4 * w + j;
        dma<0, 0>(lds + u * 1024, vx, rx, (chunk * 256 + 128 * (j & 1) + 4 * (u >> 1)) * 1152 + i * 128);
      }
      if (act) {
        const unsigned lb = lds + 32768 + w * 4096, so = p0 * 1152 + i * 128;
        dma<0, 0>(lb, rowsrc, rbr, so); dma<0, 0>(lb + 1024, rowsrc, rbr, so + 8 * 1152);
        dma<0, 0>(lb + 2048, rowsrc, rbi, so); dma<0, 0>(lb + 3072, rowsrc, rbi, so + 8 * 1152);
      }
    };
    const int p = act ? p0 + n : n;
    unsigned ulr, uli, uld;
    v2u ep;
    {
      const unsigned po = p * 2, zo = WS_EPOCH;
      asm volatile("global_load_ushort %0, %4, %5\n\tglobal_load_ushort %1, %4, %6\n\tglobal_load_ushort %2, %4, %7\n\t"
                   "global_load_dwordx2 %3, %8, %9 sc1"
                   : "=&v"(ulr), "=&v"(uli), "=&v"(uld), "=&v"(ep)
                   : "v"(po), "s"(lre), "s"(lim), "s"(ldt), "v"(zo), "s"(ws) : "memory");
    }
    _Pragma("unroll 1") for (int i = 0; i < 2; i++) a_issue(i);
    if (act) WAITV(16); else WAITV(8);
    asm volatile("" : "+v"(ulr), "+v"(uli), "+v"(uld), "+v"(ep));
    {
      const u64 t = (((u64)ep[1] << 32) | ep[0]) + 1;
      tlo = __builtin_amdgcn_readfirstlane((unsigned)t);
      thi = __builtin_amdgcn_readfirstlane((unsigned)(t >> 32));
    }
    const float lr = bflo(ulr), li = bflo(uli), lg = bflo(uld);
    float ar, ai, cr, ci;
    {
      const float dtv = rbf(expf(lg));
      const float e = expf(dtv * lr);
      float sn, cs;
      sincosf(dtv * li, &sn, &cs);
      ar = e * cs; ai = e * sn;
      const float nr = ar - 1.f, inv = 2.f / (lr * lr + li * li);
      cr = (nr * lr + ai * li) * inv; ci = (ai * lr - nr * li) * inv;
    }
    f32x4 acc[16][2];
#pragma unroll
    for (int m = 0; m < 16; m++) { acc[m][0] = f32x4{0.f, 0.f, 0.f, 0.f}; acc[m][1] = f32x4{0.f, 0.f, 0.f, 0.f}; }
    float sbr = 0.f, sbi = 0.f;
    const bf16x2 ones = __builtin_bit_cast(bf16x2, 0x3f803f80u);
    _Pragma("unroll 1") for (int i = 0; i < 9; i++) {
      if (i == 8) WAITV(0); else if (act) WAITV(8); else WAITV(4);
      BAR();
      if (act) {
        const unsigned lds = (i & 1) * 65536;
#pragma unroll
        for (int ks = 0; ks < 2; ks++) {
          const unsigned ra = lds + (ks ? rowrd1 : rowrd0);
          const v4u br = lds128(ra + 32768 + w * 4096), bi = lds128(ra + 32768 + w * 4096 + 2048);
          v4u xa[16];
#pragma unroll
          for (int m = 0; m < 16; m++) xa[m] = lds128(ra + m * 2048);
          SB();
#pragma unroll
          for (int m = 0; m < 16; m++) {
            acc[m][0] = mfma(xa[m], br, acc[m][0]);
            acc[m][1] = mfma(xa[m], bi, acc[m][1]);
          }
#pragma unroll
          for (int e = 0; e < 4; e++) {
            sbr = __builtin_amdgcn_fdot2_f32_bf16(asb(br[e]), ones, sbr, false);
            sbi = __builtin_amdgcn_fdot2_f32_bf16(asb(bi[e]), ones, sbi, false);
          }
        }
      }
      BAR();
      if (i == 4 && w < 4) dma<2, 0>(L_AFLG + w * 1024, lane * 32, rs, WS_FH + w * 2048);
      if (i == 5 && w < 2) dma<2, 0>(L_ASTAT + w * 1024, lane * 16, rs, WS_STAT + chunk * 2048 + w * 1024);
      if (i + 2 < 9) a_issue(i + 2);
    }
    if (w == 0) STAMP(4);
    if (w == 4 || w == 5) { _Pragma("unroll 1") for (int k = 0; k < 2; k++) w2_issue(k); }
    {
      bool ok = true;
#pragma unroll
      for (int j = 0; j < 4; j++) {
        const v4u f = lds128(L_AFLG + j * 1024 + lane * 16);
        ok = ok && f[0] == tlo && f[1] == thi;
      }
      if (!__all(ok)) {
        poll<4>(rs, WS_FH + lane * 32, 64 * 32, true, tlo, thi);
        if (w < 2) { dma<2, 0>(L_ASTAT + w * 1024, lane * 16, rs, WS_STAT + chunk * 2048 + w * 1024); WAITV(0); }
        BAR();
      }
    }
    if (act) {
      sbr = x32(x16(sbr)); sbi = x32(x16(sbi));
      float hr = 0.f, hi = 0.f;
#pragma unroll
      for (int m = 0; m < 16; m++) {
#pragma unroll
        for (int h = 0; h < 2; h++) {
          const f32x4 sv = __builtin_bit_cast(f32x4, lds128(L_ASTAT + (64 * g + 4 * m + 2 * h) * 8));
#pragma unroll
          for (int e = 0; e < 2; e++) {
            const int r = 2 * h + e;
            const float gr = (acc[m][0][r] - sv[2 * e] * sbr) * sv[2 * e + 1];
            const float gi2 = (acc[m][1][r] - sv[2 * e] * sbi) * sv[2 * e + 1];
            const float nr = fmaf(ar, hr, fmaf(-ai, hi, gr)), ni = fmaf(ar, hi, fmaf(ai, hr, gi2));
            hr = nr; hi = ni;
            acc[m][0][r] = hr; acc[m][1][r] = hi;
          }
        }
      }
      float pr = ar, pi = ai;
#pragma unroll
      for (int k = 0; k < 6; k++) { const float t = pr * pr - pi * pi; pi = 2.f * pr * pi; pr = t; }
      float er[4], ei[4];
#pragma unroll
      for (int k = 0; k < 4; k++) { er[k] = __shfl(hr, n + 16 * k); ei[k] = __shfl(hi, n + 16 * k); }
      {
        float tr = er[0], ti = ei[0];
#pragma unroll
        for (int k = 1; k < 4; k++) { const float t = pr * tr - pi * ti + er[k]; ti = pr * ti + pi * tr + ei[k]; tr = t; }
        if (g == 0 && chunk < 7) {
          v4u gr = {__builtin_bit_cast(unsigned, tr), tlo, __builtin_bit_cast(unsigned, ti), thi};
          __builtin_amdgcn_raw_buffer_store_b128(gr, rs, WS_AGG + (chunk * 576 + p) * 16, 0, 16);
        }
      }
      float qr = pr, qi = pi;
#pragma unroll
      for (int k = 0; k < 2; k++) { const float t = qr * qr - qi * qi; qi = 2.f * qr * qi; qr = t; }
      float Cr = 0.f, Ci = 0.f;
      if (chunk > 0) {
        v4u g0, g1;
        const bool v0 = g < chunk, v1 = g + 4 < chunk;
        _Pragma("unroll 1") for (int it = 0; it < SPIN_CAP; it++) {
          asm volatile("" ::: "memory");
          g0 = __builtin_amdgcn_raw_buffer_load_b128(rs, WS_AGG + (g * 576 + p) * 16, 0, 16);
          g1 = __builtin_amdgcn_raw_buffer_load_b128(rs, WS_AGG + ((g + 4) * 576 + p) * 16, 0, 16);
          const bool ok = (!v0 || (g0[1] == tlo && g0[3] == thi)) && (!v1 || (g1[1] == tlo && g1[3] == thi));
          if (__all(ok)) break;
          __builtin_amdgcn_s_sleep(4);
        }
        _Pragma("unroll 1") for (int j = 0; j < chunk; j++) {
          const int src = n + 16 * (j & 3);
          const float vr = __shfl(asf(j < 4 ? g0[0] : g1[0]), src);
          const float vi = __shfl(asf(j < 4 ? g0[2] : g1[2]), src);
          const float t = qr * Cr - qi * Ci + vr; Ci = qr * Ci + qi * Cr + vi; Cr = t;
        }
      }
#pragma unroll
      for (int k = 0; k < 3; k++) {
        const float tr = pr * Cr - pi * Ci + er[k], ti = pr * Ci + pi * Cr + ei[k];
        if (k < g) { Cr = tr; Ci = ti; }
      }
      float zr = cr * Cr - ci * Ci, zi = cr * Ci + ci * Cr;
      const unsigned sc = (w < 4 ? w * 8192 : 65536 + (w - 4) * 8192);
      unsigned hre[32], him[32];
#pragma unroll
      for (int m = 0; m < 16; m++) {
        float yr[4], yi[4];
#pragma unroll
        for (int r = 0; r < 4; r++) {
          const float t = ar * zr - ai * zi; zi = ar * zi + ai * zr; zr = t;
          yr[r] = fmaf(cr, acc[m][0][r], fmaf(-ci, acc[m][1][r], zr));
          yi[r] = -fmaf(cr, acc[m][1][r], fmaf(ci, acc[m][0][r], zi));
        }
        hre[2 * m] = pk2(yr[0], yr[1]); hre[2 * m + 1] = pk2(yr[2], yr[3]);
        him[2 * m] = pk2(yi[0], yi[1]); him[2 * m + 1] = pk2(yi[2], yi[3]);
      }
#pragma unroll
      for (int part = 0; part < 2; part++) {
#pragma unroll
        for (int k = 0; k < 8; k++) {
          const v4u o = part ? v4u{him[4 * k], him[4 * k + 1], him[4 * k + 2], him[4 * k + 3]}
                             : v4u{hre[4 * k], hre[4 * k + 1], hre[4 * k + 2], hre[4 * k + 3]};
          *(LAS v4u*)(size_t)(sc + lane * 128 + ((k ^ (lane & 7)) << 4)) = o;
        }
        asm volatile("" ::: "memory");
#pragma unroll
        for (int k = 0; k < 8; k++) {
          const v4u o = lds128(sc + k * 1024 + (lane >> 3) * 128 + (((lane & 7) ^ (lane >> 3)) << 4));
          __builtin_amdgcn_raw_buffer_store_b128(o, rs, (lane >> 3) * 4096 + (lane & 7) * 16,
              WS_HT + (part * 576 + p0 + 8 * (k & 1)) * 4096 + chunk * 512 + (k >> 1) * 128, 16);
        }
        asm volatile("" ::: "memory");
      }
    }
    WAITV(0);
    BAR();
    if (w == 0 && lane == 0) put_flag(rs, WS_FT + chunk * 256 + pg * 32, tlo, thi);
    if (w == 0) STAMP(5);
    if (w == 4 || w == 5) { _Pragma("unroll 1") for (int k = 2; k < 6; k++) w2_issue(k); }
  }

  if (w < 4) {
    f32x4 acc[9][2];
    if (w == q) {
      const rsrc_t rdk = mk(dsk, D * 2), rb2 = mk(b2, S * 2);
      v2u dk[9], xx[9][2], sv[2];
      unsigned bb2[2];
#pragma unroll
      for (int cc = 0; cc < 2; cc++) {
        const int s = st * 128 + 32 * w + 16 * cc + n;
        sv[cc] = __builtin_amdgcn_raw_buffer_load_b64(rs, WS_STAT + s * 8, 0, 16);
        bb2[cc] = __builtin_amdgcn_raw_buffer_load_b16(rb2, s * 2, 0, 0);
#pragma unroll
        for (int nt = 0; nt < 9; nt++) xx[nt][cc] = __builtin_amdgcn_raw_buffer_load_b64(rx, s * 1152 + (j2 * 144 + 16 * nt + 4 * g) * 2, 0, 0);
      }
#pragma unroll
      for (int nt = 0; nt < 9; nt++) dk[nt] = __builtin_amdgcn_raw_buffer_load_b64(rdk, (j2 * 144 + 16 * nt + 4 * g) * 2, 0, 0);
#pragma unroll
      for (int nt = 0; nt < 9; nt++)
#pragma unroll
        for (int cc = 0; cc < 2; cc++) {
          const float mb = asf(sv[cc][0]), r1 = asf(sv[cc][1]), bv = bflo(bb2[cc]);
#pragma unroll
          for (int h = 0; h < 2; h++) {
            const float x0 = bflo(xx[nt][cc][h]), x1 = bfhi(xx[nt][cc][h]);
            unsigned u = pk2(x0 - mb, x1 - mb);
            u = pk2(bflo(u) * r1, bfhi(u) * r1);
            u = pk2(bflo(u) * bflo(dk[nt][h]), bfhi(u) * bfhi(dk[nt][h]));
            acc[nt][cc][2 * h] = x0 + bflo(u) + bv;
            acc[nt][cc][2 * h + 1] = x1 + bfhi(u) + bv;
          }
        }
    } else {
#pragma unroll
      for (int nt = 0; nt < 9; nt++) { acc[nt][0] = f32x4{0.f, 0.f, 0.f, 0.f}; acc[nt][1] = f32x4{0.f, 0.f, 0.f, 0.f}; }
    }
    unsigned ta[2][2];
#pragma unroll
    for (int cc = 0; cc < 2; cc++)
#pragma unroll
      for (int t = 0; t < 2; t++)
        ta[cc][t] = 256 * (8 * g + 4 * t + (n >> 2)) + (((4 * w + 2 * cc + ((n & 3) >> 1)) ^ (((n >> 2) << 2) | ((2 * g + t) & 3))) << 4) + 8 * (n & 1);
    unsigned wo = 2 * 16384, fs = L_F;
    _Pragma("unroll 1") for (int k = 0; k < n2; k++) {
      BAR();
      if (k == 0 && w == 0) STAMP(7);
      const unsigned f0 = fs + (k < 18 ? lane * 16 : rowrd0), f1 = fs + (k < 18 ? lane * 16 + 1024 : rowrd1);
      v4u bb[2][2], a[2][9];
#pragma unroll
      for (int ks = 0; ks < 2; ks++) {
#pragma unroll
        for (int cc = 0; cc < 2; cc++) {
          v2u lo = ldstr(ta[cc][0] + wo + ks * 8192), hi = ldstr(ta[cc][1] + wo + ks * 8192);
          bb[ks][cc] = v4u{lo[0], lo[1], hi[0], hi[1]};
        }
#pragma unroll
        for (int nt = 0; nt < 9; nt++) a[ks][nt] = lds128((ks ? f1 : f0) + nt * 2048);
        SB();
      }
#pragma unroll
      for (int ks = 0; ks < 2; ks++)
#pragma unroll
        for (int nt = 0; nt < 9; nt++)
#pragma unroll
          for (int cc = 0; cc < 2; cc++) acc[nt][cc] = mfma(a[ks][nt], bb[ks][cc], acc[nt][cc]);
      wo = wo == 5 * 16384 ? 0 : wo + 16384;
      fs = fs == L_F + 2 * 18432 ? L_F : fs + 18432;
    }
    if (w == 0) STAMP(8);
    if (w != q) {
#pragma unroll
      for (int nt = 0; nt < 9; nt++) {
        const v4u o = {pk2(acc[nt][0][0], acc[nt][0][1]), pk2(acc[nt][0][2], acc[nt][0][3]),
                       pk2(acc[nt][1][0], acc[nt][1][1]), pk2(acc[nt][1][2], acc[nt][1][3])};
        __builtin_amdgcn_raw_buffer_store_b128(o, rs, lane * 16, WS_XS + ((tile * 4 + w) * 4 + q) * 9216 + nt * 1024, 16);
      }
      WAITV(0);
      if (lane == 0) put_flag(rs, WS_FX + (tile * 4 + w) * 128 + q * 32, tlo, thi);
      if (w == ((q + 1) & 3)) STAMP(9);
    } else {
      __builtin_amdgcn_s_sleep(16);
      poll<1>(rs, WS_FX + (tile * 4 + q) * 128 + (lane & 3) * 32, 0, (lane & 3) != q, tlo, thi);
      STAMP(10);
      v4u pv[3][9];
#pragma unroll
      for (int j = 0; j < 3; j++) {
        const int sq = (q + 1 + j) & 3;
#pragma unroll
        for (int nt = 0; nt < 9; nt++)
          pv[j][nt] = __builtin_amdgcn_raw_buffer_load_b128(rs, lane * 16, WS_XS + ((tile * 4 + q) * 4 + sq) * 9216 + nt * 1024, 16);
      }
      const rsrc_t ro = mk(out, S * D * 4);
#pragma unroll
      for (int nt = 0; nt < 9; nt++)
#pragma unroll
        for (int cc = 0; cc < 2; cc++) {
          f32x4 v = acc[nt][cc];
#pragma unroll
          for (int j = 0; j < 3; j++) {
            v[0] += bflo(pv[j][nt][2 * cc]); v[1] += bfhi(pv[j][nt][2 * cc]);
            v[2] += bflo(pv[j][nt][2 * cc + 1]); v[3] += bfhi(pv[j][nt][2 * cc + 1]);
          }
          __builtin_amdgcn_raw_buffer_store_b128(__builtin_bit_cast(v4u, v), ro, n * 2304 + g * 16,
                                                 (st * 128 + 32 * q + 16 * cc) * 2304 + (j2 * 144 + 16 * nt) * 4, 0);
        }
      STAMP(11);
    }
  } else if (w < 6) {
    WAITV(40);
    BAR();
    _Pragma("unroll 1") for (int k = 1; k < 13; k++) {
      WAITV(32);
      BAR();
      w2_issue(k + 5);
      if (k == 10 && nss) dma<2, 0>(L_MISC + wl * 1024, (lane & 7) * 32, rs, WS_FT + (st >> 1) * 256);
    }
    if (nss) {
      WAITV(16);
      const v4u f = lds128(L_MISC + wl * 1024 + lane * 16);
      if (!__all(lane >= 5 || (f[0] == tlo && f[1] == thi)))
        poll<1>(rs, WS_FT + (st >> 1) * 256 + (lane & 7) * 32, 0, (lane & 7) < 5, tlo, thi);
      if (b == 163 && w == 4 && lane == 0) put_flag(rs, WS_EPOCH, tlo, thi);
    }
    _Pragma("unroll 1") for (int k = 13; k < n2; k++) {
      const int rem = n2 - 1 - k;
      if (rem >= 4) WAITV(32); else if (rem == 3) WAITV(24); else if (rem == 2) WAITV(16); else if (rem == 1) WAITV(8); else WAITV(0);
      BAR();
      if (k + 5 < n2) {
        const unsigned lds = ((k + 7) % 6) * 16384 + wl * 8192, so = WS_HT + ((ss0 + k - 13) * 64 + 32 * wl) * 4096;
#pragma unroll
        for (int j = 0; j < 8; j++) dma<2, 0>(lds + j * 1024, vw2[j & 3], rs, so + 4 * j * 4096);
      }
    }
  } else {
    const rsrc_t rcr = mk(Cre, D * D * 2), rci = mk(Cim, D * D * 2);
    if (!isA) __builtin_amdgcn_s_sleep(POLL_DELAY_U);
    {
      const int dlo = j2 + (j2 >> 1);
      const int l18 = lane < 18 ? lane : 0;
      poll<1>(rs, WS_FU + ((9 * q + (l18 >> 1)) * 6 + dlo + (l18 & 1)) * 32, 0, true, tlo, thi);
    }
    if (w == 6) STAMP(6);
    auto f2_issue = [&](int k) {
      const unsigned lds = L_F + (k % 3) * 18432 + wl * 9216;
      if (k < 18) {
        const int sg = rl(ord2, k);
        const unsigned so = WS_UF + j2 * (72 * 18432) + (18 * q + sg) * 18432 + wl * 9216;
        dma<2, 0>(lds, lane * 16, rs, so); dma<2, 1024>(lds, lane * 16, rs, so);
        dma<2, 2048>(lds, lane * 16, rs, so); dma<2, 3072>(lds, lane * 16, rs, so);
        dma<2, 0>(lds + 4096, lane * 16, rs, so + 4096); dma<2, 1024>(lds + 4096, lane * 16, rs, so + 4096);
        dma<2, 2048>(lds + 4096, lane * 16, rs, so + 4096); dma<2, 3072>(lds + 4096, lane * 16, rs, so + 4096);
        dma<2, 0>(lds + 8192, lane * 16, rs, so + 8192);
      } else {
        const int ss = ss0 + k - 18;
        const unsigned so = (j2 * 144 + 72 * wl) * 1152 + (ss >= 9 ? ss - 9 : ss) * 128;
        if (ss >= 9) {
#pragma unroll
          for (int j = 0; j < 9; j++) dma<0, 0>(lds + j * 1024, rowsrc, rci, so + 8 * j * 1152);
        } else {
#pragma unroll
          for (int j = 0; j < 9; j++) dma<0, 0>(lds + j * 1024, rowsrc, rcr, so + 8 * j * 1152);
        }
      }
    };
    _Pragma("unroll 1") for (int k = 0; k < 3; k++) f2_issue(k);
    WAITV(18);
    BAR();
    _Pragma("unroll 1") for (int k = 1; k < n2; k++) {
      if (k + 1 < n2) WAITV(9); else WAITV(0);
      BAR();
      if (k + 2 < n2) f2_issue(k + 2);
    }
  }
}

extern "C" void kernel_launch(void* const* d_in, const int* in_sizes, int n_in, void* d_out, int out_size, void* d_ws,
                              size_t ws_size, hipStream_t stream) {
  (void)in_sizes; (void)n_in; (void)out_size; (void)ws_size;
  (void)hipFuncSetAttribute((const void*)fused_kernel, hipFuncAttributeMaxDynamicSharedMemorySize, L_TOTAL);
  fused_kernel<<<256, 512, L_TOTAL, stream>>>(
      (const u16*)d_in[0], (const u16*)d_in[1], (const u16*)d_in[2], (const u16*)d_in[3], (const u16*)d_in[4],
      (const u16*)d_in[5], (const u16*)d_in[6], (const u16*)d_in[7], (const u16*)d_in[8], (const u16*)d_in[10],
      (const u16*)d_in[11], (const u16*)d_in[12], (const u16*)d_in[13], (float*)d_out, (char*)d_ws);
}
```

```cpp
#include <hip/hip_runtime.h>

typedef unsigned short u16;
typedef unsigned long long u64;
typedef unsigned v4u __attribute__((ext_vector_type(4)));
typedef unsigned v3u __attribute__((ext_vector_type(3)));
typedef unsigned v2u __attribute__((ext_vector_type(2)));
typedef float f32x4 __attribute__((ext_vector_type(4)));
typedef float f32x2 __attribute__((ext_vector_type(2)));
typedef short s16x8 __attribute__((ext_vector_type(8)));
typedef short s16x4 __attribute__((ext_vector_type(4)));
typedef __bf16 bf16x2 __attribute__((ext_vector_type(2)));
typedef __amdgpu_buffer_rsrc_t rsrc_t;
#define LAS __attribute__((address_space(3)))
#define DEVI __device__ __forceinline__

#define TS 0
#define POLL_DELAY_H 30
#define POLL_DELAY_U 20
#define POLL_DELAY_X 2
#define STW 0
#define CODEPF 32768
#define ADELAY 8
#define POLO 0
#define FHS 32
#define FS 128
#define NE2 1
#define TOUCH 1
#define AROT 1
#define POLL_DELAY_H2 10
#define NPF 3
#define SLOW0 12
#define UDELAY 5
#define NR 6
#define LEAD1 3
#define LEAD2 3

constexpr int S = 2048, D = 576, F = 4608;
constexpr unsigned WS_EPOCH = 0, WS_FT = 16384, WS_AGG = 65536, WS_FH = 196608, WS_FU = 229376, WS_FX = 262144,
                   WS_STAT = 147456, WS_HF = 1u << 20, WS_UF = 4u << 20, WS_HT = 10u << 20, WS_XS = 16u << 20,
                   WS_TS = 32u << 20, WS_BYTES = 40u << 20;
constexpr unsigned L_F = 98304, L_MISC = 153600, L_TOTAL = 163840;
constexpr unsigned L_LN = L_F, L_SY = 131072;
constexpr unsigned L_APAR = L_MISC, L_AFLG = L_MISC + 4096, L_ASTAT = L_MISC + 5120, L_DUMMY = L_MISC + 9216;
constexpr int EPB = 16 * 8 + 1;
constexpr int SPIN_CAP = 1 << 15;

DEVI float asf(unsigned u) { return __builtin_bit_cast(float, u); }
DEVI unsigned asu(float f) { return __builtin_bit_cast(unsigned, f); }
DEVI bf16x2 asb(unsigned u) { return __builtin_bit_cast(bf16x2, u); }
DEVI float bflo(unsigned u) { return __builtin_bit_cast(float, u << 16); }
DEVI float bfhi(unsigned u) { return __builtin_bit_cast(float, u & 0xffff0000u); }
DEVI unsigned pk2(float a, float b) {
  f32x2 v = {a, b};
  return __builtin_bit_cast(unsigned, __builtin_convertvector(v, bf16x2));
}
DEVI float rbf(float a) { return bflo(pk2(a, a)); }
DEVI f32x4 mfma(v4u a, v4u b, f32x4 c) {
  return __builtin_amdgcn_mfma_f32_16x16x32_bf16(__builtin_bit_cast(s16x8, a), __builtin_bit_cast(s16x8, b), c, 0, 0, 0);
}
DEVI rsrc_t mk(const void* p, unsigned bytes) { return __builtin_amdgcn_make_buffer_rsrc((void*)p, 0, bytes, 0x00020000); }
template <int CTRL> DEVI float dadd(float s) {
  return s + __builtin_bit_cast(float, __builtin_amdgcn_update_dpp(0, __builtin_bit_cast(int, s), CTRL, 0xf, 0xf, true));
}
DEVI float x16(float s) {
  unsigned u = __builtin_bit_cast(unsigned, s);
  auto r = __builtin_amdgcn_permlane16_swap(u, u, false, false);
  return asf(r[0]) + asf(r[1]);
}
DEVI float x32(float s) {
  unsigned u = __builtin_bit_cast(unsigned, s);
  auto r = __builtin_amdgcn_permlane32_swap(u, u, false, false);
  return asf(r[0]) + asf(r[1]);
}
DEVI float red32(float s) {
  s = dadd<0xB1>(s); s = dadd<0x4E>(s); s = dadd<0x141>(s); s = dadd<0x140>(s);
  return x16(s);
}
#define SB() __builtin_amdgcn_sched_barrier(0)
#define BAR() do { SB(); asm volatile("s_waitcnt lgkmcnt(0)\n\ts_barrier" ::: "memory"); SB(); } while (0)
#define WAITV(n) asm volatile("s_waitcnt vmcnt(" #n ")" ::: "memory")
template <int POL, int OFF> DEVI void dma(unsigned lds, unsigned voff, rsrc_t r, unsigned soff) {
  if constexpr (POL == 0)
    asm volatile("s_mov_b32 m0, %0\n\ts_nop 0\n\tbuffer_load_dwordx4 %1, %2, %3 offen offset:%4 lds" ::"s"(lds), "v"(voff), "s"(r), "s"(soff), "i"(OFF) : "memory");
  else if constexpr (POL == 1)
    asm volatile("s_mov_b32 m0, %0\n\ts_nop 0\n\tbuffer_load_dwordx4 %1, %2, %3 offen offset:%4 nt lds" ::"s"(lds), "v"(voff), "s"(r), "s"(soff), "i"(OFF) : "memory");
  else
    asm volatile("s_mov_b32 m0, %0\n\ts_nop 0\n\tbuffer_load_dwordx4 %1, %2, %3 offen offset:%4 sc1 lds" ::"s"(lds), "v"(voff), "s"(r), "s"(soff), "i"(OFF) : "memory");
}
DEVI void touch(unsigned lds, unsigned voff, rsrc_t r, unsigned soff) {
  soff = __builtin_amdgcn_readfirstlane(soff);
  asm volatile("s_mov_b32 m0, %0\n\ts_nop 3\n\tbuffer_load_dword %1, %2, %3 offen lds" ::"s"(lds), "v"(voff), "s"(r), "s"(soff) : "memory");
}
#define DMA8(POLS) asm volatile("s_mov_b32 m0, %1\n\ts_nop 3\n\t" \
    "buffer_load_dwordx4 %2, %6, %0 offen " POLS "lds\n\ts_add_u32 m0, m0, 0x400\n\ts_add_u32 %0, %0, %7\n\t" \
    "buffer_load_dwordx4 %3, %6, %0 offen " POLS "lds\n\ts_add_u32 m0, m0, 0x400\n\ts_add_u32 %0, %0, %8\n\t" \
    "buffer_load_dwordx4 %4, %6, %0 offen " POLS "lds\n\ts_add_u32 m0, m0, 0x400\n\ts_add_u32 %0, %0, %7\n\t" \
    "buffer_load_dwordx4 %5, %6, %0 offen " POLS "lds\n\ts_add_u32 m0, m0, 0x400\n\ts_add_u32 %0, %0, %9\n\t" \
    "buffer_load_dwordx4 %2, %6, %0 offen " POLS "lds\n\ts_add_u32 m0, m0, 0x400\n\ts_add_u32 %0, %0, %7\n\t" \
    "buffer_load_dwordx4 %3, %6, %0 offen " POLS "lds\n\ts_add_u32 m0, m0, 0x400\n\ts_add_u32 %0, %0, %8\n\t" \
    "buffer_load_dwordx4 %4, %6, %0 offen " POLS "lds\n\ts_add_u32 m0, m0, 0x400\n\ts_add_u32 %0, %0, %7\n\t" \
    "buffer_load_dwordx4 %5, %6, %0 offen " POLS "lds" \
    : "+s"(soff) : "s"(lds), "v"(v[0]), "v"(v[1]), "v"(v[2]), "v"(v[3]), "s"(r), "i"(SA), "i"(SB_), "i"(SC) : "memory", "scc")
template <int POL, unsigned SA, unsigned SB_, unsigned SC> DEVI void dma8(unsigned lds, const unsigned (&v)[4], rsrc_t r, unsigned soff) {
  soff = __builtin_amdgcn_readfirstlane(soff); lds = __builtin_amdgcn_readfirstlane(lds);
  if constexpr (POL == 0) DMA8(""); else DMA8("sc1 ");
}
template <int N> DEVI void dmalin(unsigned lds, unsigned v, rsrc_t r, unsigned soff) {
#define L3 "buffer_load_dwordx4 %2, %3, %0 offen sc1 lds\n\tbuffer_load_dwordx4 %2, %3, %0 offen offset:1024 sc1 lds\n\t" \
           "buffer_load_dwordx4 %2, %3, %0 offen offset:2048 sc1 lds\n\t"
#define L4 L3 "buffer_load_dwordx4 %2, %3, %0 offen offset:3072 sc1 lds\n\ts_add_u32 m0, m0, 0x1000\n\ts_add_u32 %0, %0, 0x1000\n\t"
#define LGO(body) asm volatile("s_mov_b32 m0, %1\n\ts_nop 3\n\t" body : "+s"(soff) : "s"(lds), "v"(v), "s"(r) : "memory", "scc")
  soff = __builtin_amdgcn_readfirstlane(soff); lds = __builtin_amdgcn_readfirstlane(lds);
  if constexpr (N == 7) LGO(L4 L3);
  else if constexpr (N == 8) LGO(L4 L4);
  else LGO(L4 L4 "buffer_load_dwordx4 %2, %3, %0 offen sc1 lds");
#undef L3
#undef L4
#undef LGO
}
DEVI void dmarow9(unsigned lds, unsigned v, rsrc_t r, unsigned soff) {
#define R1 "buffer_load_dwordx4 %2, %3, %0 offen lds\n\ts_add_u32 m0, m0, 0x400\n\ts_add_u32 %0, %0, 0x2400\n\t"
  asm volatile("s_mov_b32 m0, %1\n\ts_nop 0\n\t" R1 R1 R1 R1 R1 R1 R1 R1 "buffer_load_dwordx4 %2, %3, %0 offen lds"
               : "+s"(soff) : "s"(lds), "v"(v), "s"(r) : "memory", "scc");
#undef R1
}
DEVI v4u lds128(unsigned a) { return *(LAS v4u*)(size_t)a; }
DEVI v2u ldstr(unsigned a) { return __builtin_bit_cast(v2u, __builtin_amdgcn_ds_read_tr16_b64_v4i16((LAS s16x4*)(size_t)a)); }
#define DSB(dst, addr, off) do { asm volatile("ds_read_b128 %0, %1 offset:%2" : "=v"(dst) : "v"(addr), "i"(off)); SB(); } while (0)
#define DST(dst, addr, off) do { asm volatile("ds_read_b64_tr_b16 %0, %1 offset:%2" : "=v"(dst) : "v"(addr), "i"(off)); SB(); } while (0)
DEVI v4u cat(v2u lo, v2u hi) { return v4u{lo[0], lo[1], hi[0], hi[1]}; }
template <int AUX> DEVI void st128_(v4u v, rsrc_t r, unsigned voff, unsigned soff) { __builtin_amdgcn_raw_buffer_store_b128(v, r, voff + soff, 0, AUX); }
#define ST128(v, r, voff, soff, aux) st128_<aux>(v, r, voff, soff)
DEVI unsigned stat_off(unsigned row) { return WS_STAT + ((row >> 6) * 32 + (row & 31)) * 16 + ((row >> 5) & 1) * 4; }
DEVI float mul(float a, float b) { float r; asm("v_mul_f32 %0, %1, %2" : "=v"(r) : "v"(a), "v"(b)); return r; }
DEVI float fma1(float a, float b, float c) { float r; asm("v_fma_f32 %0, %1, %2, %3" : "=v"(r) : "v"(a), "v"(b), "v"(c)); return r; }
DEVI float fms(float a, float b, float c) { float r; asm("v_fma_f32 %0, %1, %2, -%3" : "=v"(r) : "v"(a), "v"(b), "v"(c)); return r; }
DEVI void cfma(float& xr, float& xi, float pr, float pi, float er, float ei) {
  float tr, ti;
  asm("v_fma_f32 %0, %4, %2, %6\n\tv_fma_f32 %1, %4, %3, %7\n\tv_fma_f32 %0, -%5, %3, %0\n\tv_fma_f32 %1, %5, %2, %1"
      : "=&v"(tr), "=&v"(ti) : "v"(xr), "v"(xi), "v"(pr), "v"(pi), "v"(er), "v"(ei));
  xr = tr; xi = ti;
}
DEVI int rl(int v, int i) { return __builtin_amdgcn_readlane(v, i); }
template <int N, int M, int L, int NE> DEVI int order_table(int own, int lane) {
  auto cnt = [&](int X) { return X >= own ? (X - own) / M + 1 : 0; };
  const int cl = cnt(lane), ca = cnt(lane + L - 1 < N ? lane + L - 1 : N - 1);
  const bool mine = lane >= own && (lane - own) % M == 0;
  int rank = mine ? cl - 1 + (lane >= L && cl > NE ? lane - L + 1 - cnt(lane - L) : 0) : lane - cl + (ca > NE ? ca : NE);
  if (lane >= N || own < 0) rank = lane;
  return __builtin_amdgcn_ds_permute(rank * 4, lane);
}
template <int NL, int NAP = 4> DEVI void poll(rsrc_t rs, unsigned off, unsigned step, bool valid, unsigned tlo, unsigned thi) {
  _Pragma("unroll 1") for (int it = 0; it < SPIN_CAP; it++) {
    asm volatile("" ::: "memory");
    bool ok = true;
#pragma unroll
    for (int j = 0; j < NL; j++) {
      v2u a = __builtin_amdgcn_raw_buffer_load_b64(rs, off + j * step, 0, 16);
      ok = ok && a[0] == tlo && a[1] == thi;
    }
    if (__all(ok || !valid)) break;
    __builtin_amdgcn_s_sleep(NAP);
  }
  asm volatile("" ::: "memory");
}
DEVI void put_flag(rsrc_t rs, unsigned off, unsigned tlo, unsigned thi) {
  v2u t = {tlo, thi};
  __builtin_amdgcn_raw_buffer_store_b64(t, rs, off, 0, 16);
}
#define STAMP(i) do {} while (0)
#define STAMPX(i) do {} while (0)
#define TR() do {} while (0)
#define CYC0() do {} while (0)
#define CYC1(i) do {} while (0)
#define TDECL
#define T0() do {} while (0)
#define T1() do {} while (0)
#define T2() do {} while (0)
#define T3() do {} while (0)
#define TPUT(i) do {} while (0)

__global__ __launch_bounds__(512, 2) void fused_kernel(
    const u16* __restrict__ x, char* __restrict__ ws, const u16* __restrict__ lre, const u16* __restrict__ lim, const u16* __restrict__ Bre,
    const u16* __restrict__ Bim, const u16* __restrict__ Cre, const u16* __restrict__ Cim, const u16* __restrict__ ldt,
    const u16* __restrict__ dsk, const u16* __restrict__ w1, const u16* __restrict__ b1, const u16* __restrict__ w2,
    const u16* __restrict__ b2, float* __restrict__ out) {
  extern __shared__ __attribute__((aligned(16))) char smem[];
  u64 pc0;
  asm volatile("s_getpc_b64 %0" : "=s"(pc0));
  const int b = blockIdx.x, xcd = b & 7, idx = b >> 3, q = xcd >> 1, odd = xcd & 1;
  const int lane = threadIdx.x & 63, w = __builtin_amdgcn_readfirstlane(threadIdx.x >> 6);
  const int g = lane >> 4, n = lane & 15, wl = w & 1;
  const bool isA = idx >= (odd ? 20 : 25);
  auto code_pf = [&](unsigned lds, int piece) { dma<0, 0>(lds, lane * 16, mk((const void*)pc0, CODEPF), piece * 1024); };
  if (CODEPF && w == 7) code_pf(L_DUMMY, idx);
  asm volatile("" :: "s"(x), "s"(ws), "s"(lre), "s"(lim), "s"(Bre), "s"(Bim), "s"(Cre), "s"(Cim), "s"(ldt), "s"(dsk), "s"(w1), "s"(b1),
               "s"(w2), "s"(b2), "s"(out));
  const rsrc_t rs = mk(ws, WS_BYTES);
  const rsrc_t rx = mk(x, S * D * 2);
  TDECL;
  const int gi = (odd ? 25 : 0) + idx, grp = w >> 2, w4 = w & 3;
  const bool lnact = !isA && (grp == 0 || gi < 19);
  const int unit = q * 64 + (grp ? 45 + gi : gi);
  unsigned oa[3], tra;
  v2u tv = {0u, 0u};
  auto meet = [&](unsigned a, int lanes, bool nap = false) {
    _Pragma("unroll 1") for (int it = 0; it < (1 << 20); it++) {
      asm volatile("" ::: "memory");
      const v2u m = *(LAS v2u*)(size_t)(a + (lane & (lanes - 1)) * 8);
      if (__all(m[0] == tv[0] && m[1] == tv[1])) break;
      if (nap) __builtin_amdgcn_s_sleep(2);
    }
    asm volatile("" ::: "memory");
  };
  const int r2 = lane >> 5, c = lane & 31;
  unsigned xv[9];
  v2u ep;
  if (lnact) {
    const unsigned xo = (unit * 8 + w4 * 2 + r2) * 1152 + c * 4;
    asm volatile("buffer_load_dword %0, %10, %11, 0 offen\n\tbuffer_load_dword %1, %10, %11, 0 offen offset:128\n\t"
                 "buffer_load_dword %2, %10, %11, 0 offen offset:256\n\tbuffer_load_dword %3, %10, %11, 0 offen offset:384\n\t"
                 "buffer_load_dword %4, %10, %11, 0 offen offset:512\n\tbuffer_load_dword %5, %10, %11, 0 offen offset:640\n\t"
                 "buffer_load_dword %6, %10, %11, 0 offen offset:768\n\tbuffer_load_dword %7, %10, %11, 0 offen offset:896\n\t"
                 "buffer_load_dword %8, %10, %11, 0 offen offset:1024\n\tbuffer_load_dwordx2 %9, off, %12, 0 sc1"
                 : "=&v"(xv[0]), "=&v"(xv[1]), "=&v"(xv[2]), "=&v"(xv[3]), "=&v"(xv[4]), "=&v"(xv[5]), "=&v"(xv[6]),
                   "=&v"(xv[7]), "=&v"(xv[8]), "=&v"(ep)
                 : "v"(xo), "s"(rx), "s"(rs) : "memory");
  } else {
    asm volatile("buffer_load_dwordx2 %0, off, %1, 0 sc1" : "=&v"(ep) : "s"(rs) : "memory");
  }
  SB();
  const int ft = 9 * q + (odd ? 5 : 0) + idx / 5, dt = idx % 5;
  const int d0 = dt ? 7 * dt + 1 : 0;
  const rsrc_t rw1 = mk(w1, S * F * 2);
  unsigned vw[4];
  unsigned wslot = wl * 8192;
  auto wnext = [&]() { wslot = wslot >= 5 * 16384 ? wslot - 5 * 16384 : wslot + 16384; };
  auto vw_set = [&](unsigned rowb, unsigned col) {
#pragma unroll
    for (int jj = 0; jj < 4; jj++) vw[jj] = g * rowb + col + ((n ^ ((g << 2) | jj)) << 4);
  };
  auto w1_go = [&](unsigned so) { dma8<0, 4u * 9216u, 4u * 9216u, 4u * 9216u>(wslot, vw, rw1, so); wnext(); };
  if ((w & 6) == 4 && !isA) {
    vw_set(9216, ft * 256);
    w1_go(((dt ? dt - 1 : 0) * 64 + 32 * wl) * 9216);
  }
  const int j2 = idx & 3;
  int ord1 = 0, ord2 = 0;
  v2u bia[2];
  if (lnact) {
    f32x2 v[9];
#pragma unroll
    for (int k = 0; k < 3; k++) {
      const int d = 64 * (w4 + 4 * k) + lane;
      oa[k] = d < D ? WS_HF + (unit >> 3) * 73728 + ((unit >> 2) & 1) * 1024 + (unit & 3) * 256 + (d >> 4) * 2048 + n * 16 : 0x80000000u;
    }
    tra = L_LN + grp * 12288 + (n >> 2) * 1344 + (64 * w4 + 16 * g + 4 * (n & 3)) * 2;
    unsigned la = L_LN + grp * 12288 + (2 * w4 + r2) * 1344 + c * 4, sa = c < 2 ? stat_off(unit * 8 + w4 * 2 + r2) + c * 8 : 0x80000000u;
    asm volatile("" : "+v"(oa[0]), "+v"(oa[1]), "+v"(oa[2]), "+v"(tra), "+v"(la), "+v"(sa));
    SB();
    if ((w & 6) == 4) WAITV(8); else WAITV(0);
    asm volatile("" : "+v"(xv[0]), "+v"(xv[1]), "+v"(xv[2]), "+v"(xv[3]), "+v"(xv[4]), "+v"(xv[5]), "+v"(xv[6]), "+v"(xv[7]),
                 "+v"(xv[8]), "+v"(ep));
    {
      const unsigned bo = (ft * 128 + 32 * w4 + 4 * g) * 2;
      asm volatile("buffer_load_dwordx2 %0, %2, %3, 0 offen\n\tbuffer_load_dwordx2 %1, %2, %3, 0 offen offset:32"
                   : "=&v"(bia[0]), "=&v"(bia[1]) : "v"(bo), "s"(mk(b1, F * 2)) : "memory");
    }
#pragma unroll
    for (int j = 0; j < 9; j++) v[j] = f32x2{bflo(xv[j]), bfhi(xv[j])};
    if (w == 0) STAMPX(24);
    const f32x2 s = ((v[0] + v[1]) + (v[2] + v[3])) + ((v[4] + v[5]) + (v[6] + v[7])) + v[8];
    const float mean = red32(s[0] + s[1]) * (1.f / D);
    f32x2 qa = {0.f, 0.f}, qb = qa;
#pragma unroll
    for (int j = 0; j < 9; j++) { const f32x2 t = v[j] - mean; if (j & 1) qa += t * t; else qb += t * t; }
    qa += qb;
    const float var = red32(qa[0] + qa[1]) * (1.f / D);
    const float mb = rbf(mean), rs1 = __builtin_amdgcn_rcpf(rbf(__builtin_amdgcn_sqrtf(rbf(rbf(var) + 1e-5f))));
    f32x2 s2 = {0.f, 0.f}, q2 = {0.f, 0.f};
#pragma unroll
    for (int j = 0; j < 9; j++) {
      const f32x2 t = (v[j] - mb) * rs1;
      const unsigned u = pk2(t[0], t[1]);
      v[j] = f32x2{bflo(u), bfhi(u)};
      s2 += v[j];
      q2 += v[j] * v[j];
    }
    float s2s = s2[0] + s2[1], q2s = q2[0] + q2[1];
    s2s = dadd<0xB1>(s2s); q2s = dadd<0xB1>(q2s); s2s = dadd<0x4E>(s2s); q2s = dadd<0x4E>(q2s);
    s2s = dadd<0x141>(s2s); q2s = dadd<0x141>(q2s); s2s = dadd<0x140>(s2s); q2s = dadd<0x140>(q2s);
    const float mean2 = x16(s2s) * (1.f / D);
    const float var2 = fmaxf(x16(q2s) * (1.f / D) - mean2 * mean2, 0.f);
    const float mb2 = rbf(mean2), rs2 = __builtin_amdgcn_rcpf(rbf(__builtin_amdgcn_sqrtf(rbf(rbf(var2) + 1e-5f))));
#pragma unroll
    for (int j = 0; j < 9; j++) {
      const f32x2 t = (v[j] - mb2) * rs2;
      *(LAS unsigned*)(size_t)(la + j * 128) = pk2(t[0], t[1]);
    }
    const u64 tg = (((u64)ep[1] << 32) | ep[0]) + 1;
    tv = v2u{(unsigned)tg, (unsigned)(tg >> 32)};
    asm volatile("" ::: "memory");
    *(LAS v2u*)(size_t)(L_SY + w * 8) = tv;
    __builtin_amdgcn_raw_buffer_store_b32(asu(c ? rs1 : mb), rs, sa, 0, 16);
    if (w == 0) STAMPX(25);
    meet(L_SY + grp * 32, 4);
    v2u h0[3], h1[3];
#pragma unroll
    for (int k = 0; k < 3; k++) { h0[k] = ldstr(tra + k * 512); h1[k] = ldstr(tra + k * 512 + 4 * 1344); }
#pragma unroll
    for (int k = 0; k < 3; k++) ST128(cat(h0[k], h1[k]), rs, oa[k], 0, 16);
    if (w == 0) STAMPX(26);
    SB();
  }
  if (!isA && (w & 6) != 2) {
    const int o1 = order_table<32, 4, LEAD1, 1>(dt - 1, lane);
    ord1 = w < 6 ? (o1 * 64 + 32 * wl) * 9216 : WS_HF + o1 * 73728 + d0 * 2048 + wl * (dt ? 7168 : 8192);
    if (w < 2) vw_set(9216, ft * 256);
    asm volatile("" : "+v"(ord1), "+v"(vw[0]), "+v"(vw[1]), "+v"(vw[2]), "+v"(vw[3]));
  }
  if (lnact) {
    asm volatile("s_waitcnt vmcnt(0)" : "+v"(bia[0]), "+v"(bia[1]) :: "memory");
    if (w == 0) STAMPX(27);
    *(LAS v2u*)(size_t)(L_SY + 64 + w * 8) = tv;
    if (w4 == 0) {
      meet(L_SY + 64 + grp * 32, gi < 19 ? 4 : 8);
      __builtin_amdgcn_raw_buffer_store_b64(tv, rs, lane == 0 ? WS_FH + unit * FHS : 0x80000000u, 0, 16);
    }
    if (w == 0) STAMP(1);
    if (w < 2) {
      meet(L_SY + 64, 8);
      if (gi < 19) __builtin_amdgcn_s_sleep(2);
      wslot += 16384;
      _Pragma("unroll 1") for (int i = 1; i < 6; i++) w1_go(rl(ord1, i));
    }
    SB();
  }
  const int ai_ = odd ? idx - 13 : idx - 25;
  const int chunk = 2 * q + (ai_ >= 9 ? 1 : 0), pg = ai_ >= 9 ? ai_ - 9 : ai_;
  const unsigned rowsrc = (lane >> 3) * 1152 + (((lane & 7) ^ (lane >> 3)) << 4);
  const rsrc_t rbr = mk(Bre, D * D * 2), rbi = mk(Bim, D * D * 2);
  const unsigned vx1 = (64 * (lane >> 5) + 32 * ((lane >> 3) & 1) + ((lane >> 4) & 1)) * 1152 + (((lane & 7) ^ (lane >> 3)) << 4);
  const unsigned vx[4] = {vx1, vx1, vx1, vx1};
  auto a_ks = [&](int i) { int ks = i + AROT * pg; return (unsigned)(ks >= 9 ? ks - 9 : ks) * 128; };
  auto x_go = [&](unsigned slot, int i) {
    dma8<0, 128u * 1152u, 0u - 126u * 1152u, 0u - 126u * 1152u>(slot + (w - 4) * 8192, vx, rx, (chunk * 256 + 8 * (w - 4)) * 1152 + a_ks(i));
  };
  auto b_go = [&](unsigned slot, int i) {
    const unsigned l = slot + 32768 + (w - 4) * 4096, so = (4 * pg + w - 4) * 16 * 1152 + a_ks(i);
    dma<0, 0>(l, rowsrc, rbr, so); dma<0, 0>(l + 1024, rowsrc, rbr, so + 8 * 1152);
    dma<0, 0>(l + 2048, rowsrc, rbi, so); dma<0, 0>(l + 3072, rowsrc, rbi, so + 8 * 1152);
  };
  unsigned ulr, uli, uld;
  if (isA && w >= 4 && pg < 9) {
    {
      const int pp = pg * 64 + lane;
      const unsigned po = (pp < D ? pp : 0) * 2;
      asm volatile("global_load_ushort %0, %3, %4\n\tglobal_load_ushort %1, %3, %5\n\tglobal_load_ushort %2, %3, %6"
                   : "=&v"(ulr), "=&v"(uli), "=&v"(uld) : "v"(po), "s"(lre), "s"(lim), "s"(ldt) : "memory");
    }
    _Pragma("unroll 1") for (int i = 0; i < 2; i++) { x_go(i * 49152, i); b_go(i * 49152, i); }
    asm volatile("s_waitcnt vmcnt(24)" : "+v"(ulr), "+v"(uli), "+v"(uld) :: "memory");
  } else if (!lnact) {
    if ((w & 6) == 4 && !isA) WAITV(8); else WAITV(0);
  }
  if (!lnact) {
    asm volatile("" : "+v"(ep));
    const u64 tg = (((u64)ep[1] << 32) | ep[0]) + 1;
    tv = v2u{(unsigned)tg, (unsigned)(tg >> 32)};
    if (!isA) *(LAS v2u*)(size_t)(L_SY + 64 + w * 8) = tv;
  }
  if (w >= 4) {
    const int o2 = 18 * q + order_table<18, 4, LEAD2, NE2>(j2, lane);
    ord2 = w < 6 ? (o2 * 64 + 32 * wl) * 4096 : WS_UF + j2 * (72 * 18432) + o2 * 18432 + wl * 9216;
    asm volatile("" : "+v"(ord2));
  }
  SB();
  const int hf = odd ^ (q & 1), idx2 = (idx + (odd ? (q >> 1 ? 24 : 12) : (q >> 1 ? 28 : 0))) & 31;
  const int st = hf * 8 + (idx2 >> 2), tile = hf * 32 + idx2;
  const int qR = idx2 < 24 ? (idx2 < 12 ? 0 : 2) + (hf ^ 1) : (idx2 == 24 ? 2 : 0) + hf;
  const int nss = q == qR ? NR : (18 - NR) / 3, ss0 = q * ((18 - NR) / 3) + (q > qR ? NR - (18 - NR) / 3 : 0), n2 = nss + 18;
  const unsigned rowrd0 = n * 128 + ((g ^ (n & 7)) << 4), rowrd1 = n * 128 + (((4 + g) ^ (n & 7)) << 4);
  const rsrc_t rw2 = mk(w2, F * S * 2);
  unsigned fl = L_F + wl * 9216;
  auto fnext = [&]() { fl = fl >= L_F + 2 * 18432 ? fl - 2 * 18432 : fl + 18432; };
  auto addr = [&](int tab, int k) { unsigned so = rl(tab, k); asm volatile("" : "+s"(so)); return so; };
  auto w2_go = [&](unsigned so) { dma8<0, 16u * 4096u, 0u - 12u * 4096u, 0u - 12u * 4096u>(wslot, vw, rw2, so); wnext(); };
  auto w2_any = [&](int k) {
    if (k < nss) {
      const unsigned v1 = (lane >> 5) * 1024 + (lane & 31) * 16, vh[4] = {v1, v1, v1, v1};
      dma8<2, 2048u, 2048u, 65536u - 6144u>(wslot, vh, rs, WS_HT + ((4 * (ss0 + k) + 2 * wl) * 8 + (st >> 1)) * 8192 + (st & 1) * 512);
      wnext();
    } else w2_go(addr(ord2, k - nss));
  };
  auto c_go = [&](int k) {
    const int ss = ss0 + k;
    dmarow9(fl, rowsrc, mk(ss >= 9 ? Cim : Cre, D * D * 2), (j2 * 144 + 72 * wl) * 1152 + (ss >= 9 ? ss - 9 : ss) * 128);
    fnext();
  };
  const unsigned hflag = WS_FT + (lane >= 9 && lane < 13 && b == EPB ? (2 * (lane - 9) + 1) * 16 + 9 : (st >> 1) * 16 + (lane < 9 ? lane : 0)) * FS;
  const unsigned tlo = __builtin_amdgcn_readfirstlane(tv[0]), thi = __builtin_amdgcn_readfirstlane(tv[1]);
  if (!isA) {
    if (w < 4) {
      unsigned ta[2][2];
#pragma unroll
      for (int cc = 0; cc < 2; cc++)
#pragma unroll
        for (int t = 0; t < 2; t++)
          ta[cc][t] = 256 * (8 * g + 4 * t + (n >> 2)) + (((4 * w + 2 * cc + ((n & 3) >> 1)) ^ (((n >> 2) << 2) | ((2 * g + t) & 3))) << 4) + 8 * (n & 1);
      f32x4 acc[2][8];
#pragma unroll
      for (int cc = 0; cc < 2; cc++)
#pragma unroll
        for (int nt = 0; nt < 8; nt++) acc[cc][nt] = f32x4{bflo(bia[cc][0]), bfhi(bia[cc][0]), bflo(bia[cc][1]), bfhi(bia[cc][1])};
#pragma unroll
      for (int nt = 0; nt < 8; nt++) asm volatile("" : "+v"(acc[0][nt]), "+v"(acc[1][nt]));
      v2u t0[4], t1[4];
      unsigned wa[4], fa = L_F + lane * 16, wo = 0, fs = L_F;
#pragma unroll
      for (int j = 0; j < 4; j++) wa[j] = ta[j >> 1][j & 1];
      asm volatile("" : "+v"(wa[0]), "+v"(wa[1]), "+v"(wa[2]), "+v"(wa[3]), "+v"(fa));
      *(LAS v2u*)(size_t)(L_MISC + 2048 + w * 8) = v2u{~tlo, 0u};
      BAR();
      WAITV(0);
      BAR();
      if (w == 0) STAMP(3);
      CYC0();
#define M1(T, Fr, s_) do { acc[(s_) & 1][(s_) >> 1] = mfma(cat(T[2 * ((s_) & 1)], T[2 * ((s_) & 1) + 1]), Fr[(s_) >> 1], acc[(s_) & 1][(s_) >> 1]); SB(); } while (0)
      if (dt) {
        v4u f0[7], f1[7];
#pragma unroll
        for (int j = 0; j < 4; j++) DST(t0[j], wa[j], 0);
#pragma unroll
        for (int j = 0; j < 7; j++) DSB(f0[j], fa, j * 2048);
        _Pragma("unroll 1") for (int i = 0; i < 32; i++) {
        asm volatile("s_waitcnt lgkmcnt(5)" : "+v"(t0[0]), "+v"(t0[1]), "+v"(t0[2]), "+v"(t0[3]), "+v"(f0[0]), "+v"(f0[1])); SB();
        M1(t0, f0, 0); DST(t1[0], wa[0], 8192);
        M1(t0, f0, 1); DSB(f1[0], fa, 1024);
        M1(t0, f0, 2); DST(t1[1], wa[1], 8192);
        M1(t0, f0, 3); DSB(f1[1], fa, 1024 + 2048);
        asm volatile("s_waitcnt lgkmcnt(4)" : "+v"(f0[2]), "+v"(f0[3]), "+v"(f0[4]), "+v"(f0[5]), "+v"(f0[6])); SB();
        M1(t0, f0, 4); DST(t1[2], wa[2], 8192);
        M1(t0, f0, 5); DSB(f1[2], fa, 1024 + 2 * 2048);
        M1(t0, f0, 6); DST(t1[3], wa[3], 8192);
        M1(t0, f0, 7); DSB(f1[3], fa, 1024 + 3 * 2048);
        M1(t0, f0, 8); DSB(f1[4], fa, 1024 + 4 * 2048);
        M1(t0, f0, 9); DSB(f1[5], fa, 1024 + 5 * 2048);
        M1(t0, f0, 10); DSB(f1[6], fa, 1024 + 6 * 2048);
        wo = wo == 5 * 16384 ? 0 : wo + 16384;
        fs = fs == L_F + 2 * 18432 ? L_F : fs + 18432;
        M1(t0, f0, 11);
        wa[0] = ta[0][0] + wo; wa[1] = ta[0][1] + wo;
        asm volatile("" : "+v"(wa[0]), "+v"(wa[1])); SB();
        M1(t0, f0, 12);
        wa[2] = ta[1][0] + wo; wa[3] = ta[1][1] + wo;
        asm volatile("" : "+v"(wa[2]), "+v"(wa[3])); SB();
        M1(t0, f0, 13);
        asm volatile("s_waitcnt lgkmcnt(4)" : "+v"(t1[0]), "+v"(t1[1]), "+v"(t1[2]), "+v"(t1[3]), "+v"(f1[0])); SB();
        M1(t1, f1, 0);
        fa = fs + lane * 16;
        asm volatile("" : "+v"(fa)); SB();
        M1(t1, f1, 1);
        asm volatile("s_waitcnt lgkmcnt(0)\n\ts_barrier" : "+v"(f1[1]), "+v"(f1[2]), "+v"(f1[3]), "+v"(f1[4]), "+v"(f1[5]), "+v"(f1[6]) :: "memory"); SB();
        M1(t1, f1, 2); DST(t0[0], wa[0], 0);
        M1(t1, f1, 3); DST(t0[1], wa[1], 0);
        M1(t1, f1, 4); DSB(f0[0], fa, 0);
        M1(t1, f1, 5); DST(t0[2], wa[2], 0);
        M1(t1, f1, 6); DST(t0[3], wa[3], 0);
        M1(t1, f1, 7); DSB(f0[1], fa, 1 * 2048);
        M1(t1, f1, 8); DSB(f0[2], fa, 2 * 2048);
        M1(t1, f1, 9); DSB(f0[3], fa, 3 * 2048);
        M1(t1, f1, 10);
        M1(t1, f1, 11); DSB(f0[4], fa, 4 * 2048);
        M1(t1, f1, 12); DSB(f0[5], fa, 5 * 2048);
        M1(t1, f1, 13); DSB(f0[6], fa, 6 * 2048);
        }
        asm volatile("s_waitcnt lgkmcnt(0)" : "+v"(t0[0]), "+v"(t0[1]), "+v"(t0[2]), "+v"(t0[3]), "+v"(f0[0]), "+v"(f0[1]), "+v"(f0[2]), "+v"(f0[3]), "+v"(f0[4]), "+v"(f0[5]), "+v"(f0[6]));
        SB();
      } else {
        v4u f0[8], f1[8];
#pragma unroll
        for (int j = 0; j < 4; j++) DST(t0[j], wa[j], 0);
#pragma unroll
        for (int j = 0; j < 8; j++) DSB(f0[j], fa, j * 2048);
        _Pragma("unroll 1") for (int i = 0; i < 32; i++) {
        asm volatile("s_waitcnt lgkmcnt(6)" : "+v"(t0[0]), "+v"(t0[1]), "+v"(t0[2]), "+v"(t0[3]), "+v"(f0[0]), "+v"(f0[1])); SB();
        M1(t0, f0, 0); DST(t1[0], wa[0], 8192);
        M1(t0, f0, 1); DSB(f1[0], fa, 1024);
        M1(t0, f0, 2); DST(t1[1], wa[1], 8192);
        M1(t0, f0, 3); DSB(f1[1], fa, 1024 + 2048);
        asm volatile("s_waitcnt lgkmcnt(4)" : "+v"(f0[2]), "+v"(f0[3]), "+v"(f0[4]), "+v"(f0[5]), "+v"(f0[6]), "+v"(f0[7])); SB();
        M1(t0, f0, 4); DST(t1[2], wa[2], 8192);
        M1(t0, f0, 5); DSB(f1[2], fa, 1024 + 2 * 2048);
        M1(t0, f0, 6); DST(t1[3], wa[3], 8192);
        M1(t0, f0, 7); DSB(f1[3], fa, 1024 + 3 * 2048);
        M1(t0, f0, 8); DSB(f1[4], fa, 1024 + 4 * 2048);
        M1(t0, f0, 9); DSB(f1[5], fa, 1024 + 5 * 2048);
        M1(t0, f0, 10); DSB(f1[6], fa, 1024 + 6 * 2048);
        M1(t0, f0, 11); DSB(f1[7], fa, 1024 + 7 * 2048);
        wo = wo == 5 * 16384 ? 0 : wo + 16384;
        fs = fs == L_F + 2 * 18432 ? L_F : fs + 18432;
        M1(t0, f0, 12);
        wa[0] = ta[0][0] + wo; wa[1] = ta[0][1] + wo;
        asm volatile("" : "+v"(wa[0]), "+v"(wa[1])); SB();
        M1(t0, f0, 13);
        wa[2] = ta[1][0] + wo; wa[3] = ta[1][1] + wo;
        asm volatile("" : "+v"(wa[2]), "+v"(wa[3])); SB();
        M1(t0, f0, 14);
        M1(t0, f0, 15);
        asm volatile("s_waitcnt lgkmcnt(5)" : "+v"(t1[0]), "+v"(t1[1]), "+v"(t1[2]), "+v"(t1[3]), "+v"(f1[0])); SB();
        M1(t1, f1, 0);
        fa = fs + lane * 16;
        asm volatile("" : "+v"(fa)); SB();
        M1(t1, f1, 1);
        asm volatile("s_waitcnt lgkmcnt(0)\n\ts_barrier" : "+v"(f1[1]), "+v"(f1[2]), "+v"(f1[3]), "+v"(f1[4]), "+v"(f1[5]), "+v"(f1[6]), "+v"(f1[7]) :: "memory"); SB();
        M1(t1, f1, 2); DST(t0[0], wa[0], 0);
        M1(t1, f1, 3); DST(t0[1], wa[1], 0);
        M1(t1, f1, 4); DSB(f0[0], fa, 0);
        M1(t1, f1, 5); DST(t0[2], wa[2], 0);
        M1(t1, f1, 6); DST(t0[3], wa[3], 0);
        M1(t1, f1, 7); DSB(f0[1], fa, 1 * 2048);
        M1(t1, f1, 8); DSB(f0[2], fa, 2 * 2048);
        M1(t1, f1, 9); DSB(f0[3], fa, 3 * 2048);
        M1(t1, f1, 10);
        M1(t1, f1, 11);
        M1(t1, f1, 12); DSB(f0[4], fa, 4 * 2048);
        M1(t1, f1, 13); DSB(f0[5], fa, 5 * 2048);
        M1(t1, f1, 14); DSB(f0[6], fa, 6 * 2048);
        M1(t1, f1, 15); DSB(f0[7], fa, 7 * 2048);
        }
        asm volatile("s_waitcnt lgkmcnt(0)" : "+v"(t0[0]), "+v"(t0[1]), "+v"(t0[2]), "+v"(t0[3]), "+v"(f0[0]), "+v"(f0[1]), "+v"(f0[2]), "+v"(f0[3]), "+v"(f0[4]), "+v"(f0[5]), "+v"(f0[6]), "+v"(f0[7]));
        SB();
      }
      if (w == 0) CYC1(29);
      if (w == 0) STAMP(4);
#pragma unroll
      for (int nt = 0; nt < 8; nt++) {
        if (nt == 7 && dt) break;
        v4u o;
#pragma unroll
        for (int cc = 0; cc < 2; cc++)
#pragma unroll
          for (int h = 0; h < 2; h++) {
            const f32x2 t = {acc[cc][nt][2 * h], acc[cc][nt][2 * h + 1]};
            const f32x2 z = t * (t * t * -0.1029432397f + -2.302208198f);
            f32x2 e = {__builtin_amdgcn_exp2f(z[0]), __builtin_amdgcn_exp2f(z[1])};
            e += 1.f;
            const f32x2 y = t * f32x2{__builtin_amdgcn_rcpf(e[0]), __builtin_amdgcn_rcpf(e[1])};
            o[2 * cc + h] = pk2(y[0], y[1]);
          }
        const int d16 = d0 + nt, jj = d16 / 9, nt2 = d16 - 9 * jj;
        ST128(o, rs, lane * 16, WS_UF + jj * (72 * 18432) + (2 * ft + (w >> 1)) * 18432 + nt2 * 2048 + (w & 1) * 1024, 16);
        SB();
      }
    } else if (w < 6) {
      wslot = wl * 8192;
      BAR();
      WAITV(0);
      BAR();
      _Pragma("unroll 1") for (int i = 6; i < 32; i++) {
        const unsigned so = addr(ord1, i);
        WAITV(32);
        BAR();
        w1_go(so);
      }
      vw_set(4096, st * 256);
      WAITV(32); BAR();
      WAITV(24); BAR();
      dma<2, 0>(L_MISC + wl * 1024, hflag, rs, 0);
      WAITV(17); BAR();
      WAITV(0);
      const v4u f = lds128(L_MISC + wl * 1024 + lane * 16);
      if (__builtin_amdgcn_readfirstlane(__all(f[0] == tlo && f[1] == thi))) {
        _Pragma("unroll 1") for (int k = 0; k < 4; k += 2) {
          BAR();
          w2_any(k);
          w2_any(k + 1);
        }
        BAR();
      } else {
        BAR(); BAR(); BAR();
        poll<1>(rs, hflag, 0, true, tlo, thi);
        _Pragma("unroll 1") for (int k = 0; k < 4; k++) w2_any(k);
      }
    } else {
      meet(L_SY + 64, 4, true);
      if (lnact) __builtin_amdgcn_s_sleep(POLL_DELAY_H2); else __builtin_amdgcn_s_sleep(POLL_DELAY_H);
      poll<2>(rs, WS_FH + (wl * 128 + lane) * FHS, 64 * FHS, true, tlo, thi);
      if (w == 6) STAMP(2);
      BAR();
      unsigned l1 = L_F + wl * (dt ? 7168 : 8192);
      auto f1_go = [&](unsigned so) {
        if (dt) dmalin<7>(l1, lane * 16, rs, so); else dmalin<8>(l1, lane * 16, rs, so);
        l1 = l1 >= L_F + 2 * 18432 ? l1 - 2 * 18432 : l1 + 18432;
      };
      _Pragma("unroll 1") for (int i = 0; i < 3; i++) f1_go(addr(ord1, i));
      if (dt) WAITV(14); else WAITV(16);
      BAR(); TR();
      _Pragma("unroll 1") for (int i = 3; i < 32; i++) {
        const unsigned so = addr(ord1, i);
        if (dt) WAITV(7); else WAITV(8);
        BAR();
        TR();
        f1_go(so);
      }
      WAITV(7);
      BAR(); TR();
      WAITV(0);
      BAR(); TR();
      BAR(); TR();
    }
  } else {
    const bool act = w < 4 && pg < 9;
    const int pt = 4 * pg + w;
    f32x4 acc[16][2];
    f32x4 sb0 = {0.f, 0.f, 0.f, 0.f}, sb1 = sb0;
    auto hfo = [&]() { return WS_FH + (lane < 32 ? chunk * 32 + lane : st * 16 + 4 * q + (lane & 3)) * FHS; };
    if (pg == 9) {
      const unsigned tw = (lane >> 1) * 9216 + (lane & 1) * 128;
      const int fs0 = (1 - 9 * q - (int)((size_t)w1 >> 8)) & 3, ns = fs0 == 0 ? 3 : 2;
      _Pragma("unroll 1") for (int t = w; t < 2 * (32 - 4 * NPF - SLOW0) * ns; t += 8) {
        touch(L_DUMMY, tw, rw1, ((2 * SLOW0 + t / ns) * 32) * 9216 + (9 * q + fs0 + 4 * (t % ns)) * 256);
        WAITV(0);
      }
      if (w == 7) dma<2, 0>(L_AFLG, hfo(), rs, 0);
      WAITV(0);
      BAR();
    } else if (w < 4) {
#pragma unroll
      for (int m = 0; m < 16; m++) { acc[m][0] = f32x4{0.f, 0.f, 0.f, 0.f}; acc[m][1] = f32x4{0.f, 0.f, 0.f, 0.f}; }
#pragma unroll
      for (int m = 0; m < 16; m++) asm volatile("" : "+v"(acc[m][0]), "+v"(acc[m][1]));
      BAR();
      STAMPX(24);
      constexpr int PD = 7;
      v4u xr[8], bq[2][2];
      const v4u ones = {0x3f803f80u, 0x3f803f80u, 0x3f803f80u, 0x3f803f80u};
      const unsigned bo = 32768 + w * 4096;
      bq[0][0] = lds128(rowrd0 + bo); bq[0][1] = lds128(rowrd0 + bo + 2048);
#pragma unroll
      for (int j = 0; j < PD; j++) xr[j] = lds128(rowrd0 + j * 2048);
      SB();
      unsigned xc = 0;
      _Pragma("unroll 1") for (int i = 0; i < 9; i++) {
        const unsigned r0 = xc + rowrd0, r1 = xc + rowrd1;
        xc = xc == 2 * 49152 ? 0 : xc + 49152;
        const unsigned rn = xc + rowrd0;
#pragma unroll
        for (int j = 0; j < 32 - PD; j++) {
          const int jn = j + PD;
          xr[jn & 7] = lds128((jn < 16 ? r0 : r1) + (jn & 15) * 2048);
          if (j == 4) bq[1][0] = lds128(r1 + bo);
          if (j == 5) bq[1][1] = lds128(r1 + bo + 2048);
          acc[j & 15][0] = mfma(xr[j & 7], bq[j >> 4][0], acc[j & 15][0]);
          acc[j & 15][1] = mfma(xr[j & 7], bq[j >> 4][1], acc[j & 15][1]);
          if (j == 5 || j == 32 - PD - 1) { sb0 = mfma(ones, bq[j >> 4][0], sb0); sb1 = mfma(ones, bq[j >> 4][1], sb1); }
          SB();
        }
        BAR();
#pragma unroll
        for (int j = 32 - PD; j < 32; j++) {
          const int jn = j + PD - 32;
          if (jn == 0) { bq[0][0] = lds128(rn + bo); bq[0][1] = lds128(rn + bo + 2048); }
          xr[jn & 7] = lds128(rn + jn * 2048);
          acc[j & 15][0] = mfma(xr[j & 7], bq[1][0], acc[j & 15][0]);
          acc[j & 15][1] = mfma(xr[j & 7], bq[1][1], acc[j & 15][1]);
          SB();
        }
      }
    } else {
      WAITV(12);
      BAR();
      unsigned xw = 2 * 49152;
      _Pragma("unroll 1") for (int i = 2; i < 9; i++) {
        if (i == 7 && w == 7) dma<2, 0>(L_AFLG, hfo(), rs, 0);
        if (i == 8 && w < 6) dma<2, 0>(L_ASTAT + wl * 1024, lane * 16, rs, WS_STAT + chunk * 2048 + wl * 1024);
        x_go(xw, i); b_go(xw, i);
        xw = xw == 2 * 49152 ? 0 : xw + 49152;
        WAITV(12);
        BAR();
      }
      if (w == 7) {
        const float lr = bflo(ulr), li = bflo(uli), dtv = rbf(expf(bflo(uld)));
        const float e = expf(dtv * lr);
        float sn, cs;
        sincosf(dtv * li, &sn, &cs);
        const float ar = mul(e, cs), ai = mul(e, sn);
        const float nr = fms(e, cs, 1.f), den = fma1(lr, lr, mul(li, li));
        float rc;
        asm("v_rcp_f32 %0, %1\n\ts_nop 1" : "=v"(rc) : "v"(den));
        const float inv = 2.f * fms(rc, 1.f, mul(fms(den, rc, 1.f), rc));
        const float cr = mul(fma1(nr, lr, mul(ai, li)), inv), ci = mul(fms(ai, lr, mul(nr, li)), inv);
        float pr = ar, pi = ai;
#pragma unroll
        for (int k = 0; k < 5; k++) { const float t = fms(pr, pr, mul(pi, pi)); pi = mul(pr + pr, pi); pr = t; }
        float qr = pr, qi = pi;
#pragma unroll
        for (int k = 0; k < 3; k++) { const float t = fms(qr, qr, mul(qi, qi)); qi = mul(qr + qr, qi); qr = t; }
        const unsigned pa = L_APAR + lane * 32;
        *(LAS f32x4*)(size_t)pa = f32x4{ar, ai, cr, ci};
        *(LAS f32x4*)(size_t)(pa + 16) = f32x4{pr, pi, qr, qi};
      }

      WAITV(0);
      const int t = ai_ * 4 + (w - 4);
      const unsigned tw = (lane >> 1) * 9216 + (lane & 1) * 128, to = ((64 - 8 * NPF + t / 9) * 32) * 9216 + (9 * q + t % 9) * 256;
      if (NPF > 0) touch(L_DUMMY, tw, rw1, to);
      BAR();
      if (NPF > 1) touch(L_DUMMY, tw, rw1, to + 256 * 9216);
      BAR();
#pragma unroll
      for (int j = 2; j < NPF; j++) touch(L_DUMMY, tw, rw1, to + j * 256 * 9216);
    }
    if (w == 0) STAMP(4);
    {
      const v4u f = lds128(L_AFLG + lane * 16);
      if (!__all(f[0] == tlo && f[1] == thi)) {
        poll<1>(rs, hfo(), 0, true, tlo, thi);
        if (w < 2) { dma<2, 0>(L_ASTAT + w * 1024, lane * 16, rs, WS_STAT + chunk * 2048 + w * 1024); WAITV(0); }
        BAR();
      }
    }
    if (act) {
      STAMPX(25);
      const f32x4 pa0 = *(LAS f32x4*)(size_t)(L_APAR + (w * 16 + n) * 32), pa1 = *(LAS f32x4*)(size_t)(L_APAR + (w * 16 + n) * 32 + 16);
      f32x2 SR = {sb0[0], sb0[1]}, SI = {sb1[0], sb1[1]}, AR = {pa0[0], pa0[0]}, AI = {pa0[1], pa0[1]}, CR = {pa0[2], pa0[2]}, CI = {pa0[3], pa0[3]};
      asm volatile("" : "+v"(AR), "+v"(AI), "+v"(CR), "+v"(CI));
      f32x2 P[32], Q[32];
#pragma unroll
      for (int j = 0; j < 32; j++) {
        P[j] = f32x2{acc[j >> 1][0][2 * (j & 1)], acc[j >> 1][0][2 * (j & 1) + 1]};
        Q[j] = f32x2{acc[j >> 1][1][2 * (j & 1)], acc[j >> 1][1][2 * (j & 1) + 1]};
      }
      f32x2 HR = {0.f, 0.f}, HI = {0.f, 0.f};
      f32x4 sv[2][4];
      const unsigned sa = L_ASTAT + g * 512;
#pragma unroll
      for (int j = 0; j < 4; j++) sv[0][j] = __builtin_bit_cast(f32x4, lds128(sa + j * 16));
#pragma unroll
      for (int mb = 0; mb < 8; mb++) {
        if (mb < 7) {
#pragma unroll
          for (int j = 0; j < 4; j++) sv[(mb + 1) & 1][j] = __builtin_bit_cast(f32x4, lds128(sa + (mb + 1) * 64 + j * 16));
        }
        SB();
#pragma unroll
        for (int j = 0; j < 4; j += 2) {
          const int k = 4 * mb + j;
          const f32x4 s0 = sv[mb & 1][j], s1 = sv[mb & 1][j + 1];
          const f32x2 M0 = {s0[0], s0[1]}, R0 = {s0[2], s0[3]}, M1 = {s1[0], s1[1]}, R1 = {s1[2], s1[3]};
#define NEGA " neg_lo:[1,0,0] neg_hi:[1,0,0]"
          asm("v_pk_fma_f32 %0, %4, %8, %0" NEGA "\n\tv_pk_fma_f32 %1, %4, %9, %1" NEGA "\n\t"
              "v_pk_fma_f32 %2, %6, %8, %2" NEGA "\n\tv_pk_fma_f32 %3, %6, %9, %3" NEGA "\n\t"
              "v_pk_mul_f32 %0, %0, %5\n\tv_pk_mul_f32 %1, %1, %5\n\tv_pk_mul_f32 %2, %2, %7\n\t"
              "v_pk_fma_f32 %0, %11, %13, %0" NEGA "\n\tv_pk_fma_f32 %1, %11, %12, %1\n\tv_pk_mul_f32 %3, %3, %7\n\t"
              "v_pk_fma_f32 %0, %10, %12, %0\n\tv_pk_fma_f32 %1, %10, %13, %1\n\t"
              "v_pk_fma_f32 %2, %10, %0, %2\n\tv_pk_fma_f32 %3, %11, %0, %3\n\t"
              "v_pk_fma_f32 %2, %11, %1, %2" NEGA "\n\tv_pk_fma_f32 %3, %10, %1, %3"
              : "+v"(P[k]), "+v"(Q[k]), "+v"(P[k + 1]), "+v"(Q[k + 1])
              : "v"(M0), "v"(R0), "v"(M1), "v"(R1), "v"(SR), "v"(SI), "v"(AR), "v"(AI), "v"(HR), "v"(HI));
          HR = P[k + 1]; HI = Q[k + 1];
        }
        SB();
      }
      STAMPX(26);
      const float l32r = pa1[0], l32i = pa1[1], qr = pa1[2], qi = pa1[3];
      const float pr = fms(l32r, l32r, mul(l32i, l32i)), pi = mul(l32r + l32r, l32i);
      float hr = HR[0], hi = HI[0];
      cfma(hr, hi, l32r, l32i, HR[1], HI[1]);
      float er[4], ei[4];
#pragma unroll
      for (int k = 0; k < 4; k++) { er[k] = __shfl(hr, n + 16 * k); ei[k] = __shfl(hi, n + 16 * k); }
      const unsigned ag = WS_AGG + (pt * 16 + n) * 16;
      {
        float tr = er[0], ti = ei[0];
#pragma unroll
        for (int k = 1; k < 4; k++) cfma(tr, ti, pr, pi, er[k], ei[k]);
        const v4u gr = {asu(tr), tlo, asu(ti), thi};
        ST128(gr, rs, g == 0 ? ag + chunk * (576 * 16) : 0x80000000u, 0, 16);
      }
      float Cr = 0.f, Ci = 0.f;
      if (chunk > 0) {
        v4u ga[7];
        __builtin_amdgcn_s_sleep(ADELAY);
        _Pragma("unroll 1") for (int it = 0; it < SPIN_CAP; it++) {
          asm volatile("" ::: "memory");
#pragma unroll
          for (int j = 0; j < 7; j++)
            ga[j] = __builtin_amdgcn_raw_buffer_load_b128(rs, (ag + (chunk - 7 + j) * (576 * 16)) | (chunk - 7 + j < 0 ? 0x80000000u : 0u), 0, 16);
          SB();
          unsigned bad = 0;
#pragma unroll
          for (int j = 0; j < 7; j++) bad |= ((ga[j][1] ^ tlo) | (ga[j][3] ^ thi)) & (chunk - 7 + j < 0 ? 0u : 0xffffffffu);
          if (__all(bad == 0)) break;
          __builtin_amdgcn_s_sleep(4);
        }
#pragma unroll
        for (int j = 0; j < 7; j++) cfma(Cr, Ci, qr, qi, asf(ga[j][0]), asf(ga[j][2]));
      }
#pragma unroll
      for (int k = 0; k < 3; k++) {
        float tr = Cr, ti = Ci;
        cfma(tr, ti, pr, pi, er[k], ei[k]);
        if (k < g) { Cr = tr; Ci = ti; }
      }
      float Dr = Cr, Di = Ci;
      cfma(Dr, Di, l32r, l32i, HR[0], HI[0]);
      cfma(Cr, Ci, CR[0], CI[0], 0.f, 0.f);
      cfma(Dr, Di, CR[0], CI[0], 0.f, 0.f);
      f32x2 ZR = {Cr, Dr}, ZI = {-Ci, -Di};
      asm volatile("" : "+v"(ZR), "+v"(ZI));
      STAMPX(27);
      const unsigned ha = WS_HT + (pt * 8 + chunk) * 8192 + (g >> 1) * 512 + ((n >> 2) & 1) * 256 + (n >> 3) * 128 + (g & 1) * 64 + (n & 3) * 16;
#pragma unroll
      for (int kk = 0; kk < 4; kk++) {
        v4u ore[2], oim[2];
#pragma unroll
        for (int jj = 0; jj < 4; jj++) {
          const int k = 8 * kk + 2 * jj;
          f32x2 A0, B0, A1, B1;
          asm("v_pk_mul_f32 %2, %10, %0\n\tv_pk_mul_f32 %3, %10, %1\n\t"
              "v_pk_fma_f32 %2, %11, %1, %2\n\tv_pk_fma_f32 %3, %11, %0, %3" NEGA "\n\t"
              "v_pk_mul_f32 %0, %10, %2\n\tv_pk_mul_f32 %1, %10, %3\n\t"
              "v_pk_fma_f32 %0, %11, %3, %0\n\tv_pk_fma_f32 %1, %11, %2, %1" NEGA "\n\t"
              "v_pk_fma_f32 %2, %12, %6, %2\n\tv_pk_fma_f32 %3, %12, %7, %3" NEGA "\n\t"
              "v_pk_fma_f32 %4, %12, %8, %0\n\tv_pk_fma_f32 %5, %12, %9, %1" NEGA "\n\t"
              "v_pk_fma_f32 %2, %13, %7, %2" NEGA "\n\tv_pk_fma_f32 %3, %13, %6, %3" NEGA "\n\t"
              "v_pk_fma_f32 %4, %13, %9, %4" NEGA "\n\tv_pk_fma_f32 %5, %13, %8, %5" NEGA
              : "+v"(ZR), "+v"(ZI), "=&v"(A0), "=&v"(B0), "=&v"(A1), "=&v"(B1)
              : "v"(P[k]), "v"(Q[k]), "v"(P[k + 1]), "v"(Q[k + 1]), "v"(AR), "v"(AI), "v"(CR), "v"(CI));
          ore[0][jj] = pk2(A0[0], A1[0]); ore[1][jj] = pk2(A0[1], A1[1]);
          oim[0][jj] = pk2(B0[0], B1[0]); oim[1][jj] = pk2(B0[1], B1[1]);
        }
#pragma unroll
        for (int u = 0; u < 2; u++) {
          ST128(ore[u], rs, ha ^ ((kk & 1) * 64), (4 * u + kk) * 1024, 16);
          ST128(oim[u], rs, ha ^ ((kk & 1) * 64), (4 * u + kk) * 1024 + 36 * 65536, 16);
        }
        SB();
      }
    }
    STAMPX(28);
    WAITV(0);
    STAMPX(29);
    BAR();
    if (w == 0 && lane == 0) put_flag(rs, WS_FT + (chunk * 16 + pg) * FS, tlo, thi);
    if (w == 0) STAMP(5);
    if (pg < 9) {
      if (w < 2 || (w < 4 && q == 0 && ai_ < 9)) touch(L_DUMMY, lane * 128, mk(wl ? Cim : Cre, D * D * 2), (w < 2 ? q * 18 + ai_ : 72 + ai_) * 8192);
      const int t = w * 18 + ai_;
      if (TOUCH & 1) touch(L_DUMMY, lane * 128, rw2, 1152 * q * 4096 + t * 8192);
      if (TOUCH & 2) touch(L_DUMMY, (lane >> 1) * 4096 + (lane & 1) * 128, rw2, (1152 * q + (t >> 2) * 32) * 4096 + (((1 - (int)((size_t)w2 >> 8)) & 3) + 4 * (t & 3)) * 256);
    }
    wslot += 2 * 16384;
    if ((w & 6) == 4) {
      vw_set(4096, st * 256);
      poll<1, 16>(rs, hflag, 0, true, tlo, thi);
      _Pragma("unroll 1") for (int k = 0; k < 4; k++) w2_any(k);
    }
  }

  if (w < 4) {
    f32x4 acc[9][2];
#pragma unroll
    for (int nt = 0; nt < 9; nt++) { acc[nt][0] = f32x4{0.f, 0.f, 0.f, 0.f}; acc[nt][1] = f32x4{0.f, 0.f, 0.f, 0.f}; }
#pragma unroll
    for (int nt = 0; nt < 9; nt++) asm volatile("" : "+v"(acc[nt][0]), "+v"(acc[nt][1]));
    unsigned ta[2][2];
#pragma unroll
    for (int cc = 0; cc < 2; cc++)
#pragma unroll
      for (int t = 0; t < 2; t++)
        ta[cc][t] = (g >> 1) * 4096 + (4 * (w & 1) + 2 * cc + ((n & 3) >> 1)) * 512 + t * 256 + (g & 1) * 128 +
                        ((w >> 1) ^ ((n & 3) >> 1)) * 64 + (n >> 2) * 16 + 8 * (n & 1);
    v2u t0[4], t1[4];
    v4u f0[9], f1[9];
    unsigned wo = 2 * 16384, fs = L_F;
    unsigned wa[4], l0 = rowrd0, l1 = rowrd1, fa0 = fs + l0, fa1 = fs + l1;
#pragma unroll
    for (int j = 0; j < 4; j++) wa[j] = ta[j >> 1][j & 1] + wo;
    asm volatile("" : "+v"(wa[0]), "+v"(wa[1]), "+v"(wa[2]), "+v"(wa[3]), "+v"(fa0), "+v"(fa1));
    WAITV(0);
    if (!isA) {
      *(LAS v2u*)(size_t)(L_MISC + 2048 + w * 8) = tv;
      if (w == 0) {
        meet(L_MISC + 2048, 4);
        if (lane == 0) put_flag(rs, WS_FU + (ft * 5 + dt) * FS, tlo, thi);
        STAMP(5);
      }
    }
    BAR();
    if (w == 0) STAMP(7);
    CYC0();
#pragma unroll
    for (int j = 0; j < 4; j++) DST(t0[j], wa[j], 0);
#pragma unroll
    for (int j = 0; j < 9; j++) DSB(f0[j], fa0, j * 2048);
#define M2(T, Fr, s_) do { acc[(s_) >> 1][(s_) & 1] = mfma(Fr[(s_) >> 1], cat(T[2 * ((s_) & 1)], T[2 * ((s_) & 1) + 1]), acc[(s_) >> 1][(s_) & 1]); SB(); } while (0)
    _Pragma("unroll 1") for (int k = 0; k < n2; k++) {
      if (k == nss - 1) {
#pragma unroll
        for (int cc = 0; cc < 2; cc++)
#pragma unroll
          for (int t = 0; t < 2; t++)
            ta[cc][t] = 256 * (8 * g + 4 * t + (n >> 2)) + (((4 * w + 2 * cc + ((n & 3) >> 1)) ^ (((n >> 2) << 2) | ((2 * g + t) & 3))) << 4) + 8 * (n & 1);
        l0 = lane * 16; l1 = lane * 16 + 1024;
        asm volatile("" : "+v"(ta[0][0]), "+v"(ta[0][1]), "+v"(ta[1][0]), "+v"(ta[1][1]), "+v"(l0), "+v"(l1));
      }
      SB();
      asm volatile("s_waitcnt lgkmcnt(6)" : "+v"(t0[0]), "+v"(t0[1]), "+v"(t0[2]), "+v"(t0[3]), "+v"(f0[0]), "+v"(f0[1]), "+v"(f0[2])); SB();
      M2(t0, f0, 0); DST(t1[0], wa[0], 8192);
      M2(t0, f0, 1); DSB(f1[0], fa1, 0);
      M2(t0, f0, 2); DST(t1[1], wa[1], 8192);
      M2(t0, f0, 3); DSB(f1[1], fa1, 2048);
      M2(t0, f0, 4); DST(t1[2], wa[2], 8192);
      M2(t0, f0, 5);
      asm volatile("s_waitcnt lgkmcnt(5)" : "+v"(f0[3]), "+v"(f0[4]), "+v"(f0[5]), "+v"(f0[6]), "+v"(f0[7]), "+v"(f0[8])); SB();
      M2(t0, f0, 6); DSB(f1[2], fa1, 2 * 2048);
      M2(t0, f0, 7); DST(t1[3], wa[3], 8192);
      M2(t0, f0, 8); DSB(f1[3], fa1, 3 * 2048);
      M2(t0, f0, 9); DSB(f1[4], fa1, 4 * 2048);
      wo = wo == 5 * 16384 ? 0 : wo + 16384;
      M2(t0, f0, 10);
      wa[0] = ta[0][0] + wo; wa[1] = ta[0][1] + wo;
      asm volatile("" : "+v"(wa[0]), "+v"(wa[1])); SB();
      M2(t0, f0, 11); DSB(f1[5], fa1, 5 * 2048);
      M2(t0, f0, 12); DSB(f1[6], fa1, 6 * 2048);
      fs = fs == L_F + 2 * 18432 ? L_F : fs + 18432;
      M2(t0, f0, 13);
      wa[2] = ta[1][0] + wo; wa[3] = ta[1][1] + wo;
      asm volatile("" : "+v"(wa[2]), "+v"(wa[3])); SB();
      M2(t0, f0, 14); DSB(f1[7], fa1, 7 * 2048);
      M2(t0, f0, 15); DSB(f1[8], fa1, 8 * 2048);
      M2(t0, f0, 16);
      M2(t0, f0, 17);
      asm volatile("s_waitcnt lgkmcnt(6)" : "+v"(t1[0]), "+v"(t1[1]), "+v"(t1[2]), "+v"(t1[3]), "+v"(f1[0]), "+v"(f1[1])); SB();
      M2(t1, f1, 0);
      fa0 = fs + l0; fa1 = fs + l1;
      asm volatile("" : "+v"(fa0), "+v"(fa1)); SB();
      M2(t1, f1, 1);
      M2(t1, f1, 2);
      asm volatile("s_waitcnt lgkmcnt(0)\n\ts_barrier" : "+v"(f1[2]), "+v"(f1[3]), "+v"(f1[4]), "+v"(f1[5]), "+v"(f1[6]), "+v"(f1[7]), "+v"(f1[8]) :: "memory"); SB();
      M2(t1, f1, 3); DST(t0[0], wa[0], 0);
      M2(t1, f1, 4); DST(t0[1], wa[1], 0);
      M2(t1, f1, 5); DSB(f0[0], fa0, 0);
      M2(t1, f1, 6); DST(t0[2], wa[2], 0);
      M2(t1, f1, 7); DSB(f0[1], fa0, 2048);
      M2(t1, f1, 8); DST(t0[3], wa[3], 0);
      M2(t1, f1, 9); DSB(f0[2], fa0, 2 * 2048);
      M2(t1, f1, 10); DSB(f0[3], fa0, 3 * 2048);
      M2(t1, f1, 11);
      M2(t1, f1, 12); DSB(f0[4], fa0, 4 * 2048);
      M2(t1, f1, 13); DSB(f0[5], fa0, 5 * 2048);
      M2(t1, f1, 14);
      M2(t1, f1, 15); DSB(f0[6], fa0, 6 * 2048);
      M2(t1, f1, 16); DSB(f0[7], fa0, 7 * 2048);
      M2(t1, f1, 17); DSB(f0[8], fa0, 8 * 2048);
    }
    asm volatile("s_waitcnt lgkmcnt(0)" : "+v"(t0[0]), "+v"(t0[1]), "+v"(t0[2]), "+v"(t0[3]), "+v"(f0[0]), "+v"(f0[1]),
                 "+v"(f0[2]), "+v"(f0[3]), "+v"(f0[4]), "+v"(f0[5]), "+v"(f0[6]), "+v"(f0[7]), "+v"(f0[8]));
    SB();
    if (w == 0) TPUT(21);
    if (w == 0) CYC1(23);
    if (w == 0) STAMP(8);
    if (w != q) {
#pragma unroll
      for (int nt = 0; nt < 9; nt++) {
        const v4u o = {pk2(acc[nt][0][0], acc[nt][0][1]), pk2(acc[nt][0][2], acc[nt][0][3]),
                       pk2(acc[nt][1][0], acc[nt][1][1]), pk2(acc[nt][1][2], acc[nt][1][3])};
        ST128(o, rs, lane * 16, WS_XS + ((tile * 4 + w) * 4 + q) * 9216 + nt * 1024, 16);
      }
      WAITV(0);
      if (lane == 0) put_flag(rs, WS_FX + ((tile * 4 + w) * 4 + q) * FS, tlo, thi);
      if (w == ((q + 1) & 3)) STAMP(9);
    } else {
#pragma unroll
      for (int nt = 0; nt < 9; nt++) {
        *(LAS f32x4*)(size_t)(nt * 2048 + lane * 16) = acc[nt][0];
        *(LAS f32x4*)(size_t)(nt * 2048 + 1024 + lane * 16) = acc[nt][1];
      }
      asm volatile("" ::: "memory");
      *(LAS v2u*)(size_t)24576 = tv;
    }
  } else {
    const int lw = w - 4;
    v2u xx[3][2], dk[3];
    v3u sv[2];
    unsigned bb2[2];
    auto base_in = [&]() {
      int l = lane;
      asm volatile("" : "+v"(l));
      const unsigned s0 = st * 128 + 32 * q + (l & 15);
      unsigned xo[3], co[3];
#pragma unroll
      for (int u = 0; u < 3; u++) {
        co[u] = ((j2 * 144 + 16 * (lw + 4 * u) + 4 * (l >> 4)) * 2) | ((u < 2 || lw == 0) ? 0u : 0x40000000u);
        xo[u] = s0 * 1152 + co[u];
      }
      const unsigned r16 = 16 * 1152, so = stat_off(s0), bo = s0 * 2;
      const rsrc_t rdk = mk(dsk, D * 2), rb2 = mk(b2, S * 2);
      asm volatile("buffer_load_dwordx2 %0, %6, %9, 0 offen\n\tbuffer_load_dwordx2 %1, %6, %9, %10 offen\n\t"
                   "buffer_load_dwordx2 %2, %7, %9, 0 offen\n\tbuffer_load_dwordx2 %3, %7, %9, %10 offen\n\t"
                   "buffer_load_dwordx2 %4, %8, %9, 0 offen\n\tbuffer_load_dwordx2 %5, %8, %9, %10 offen"
                   : "=&v"(xx[0][0]), "=&v"(xx[0][1]), "=&v"(xx[1][0]), "=&v"(xx[1][1]), "=&v"(xx[2][0]), "=&v"(xx[2][1])
                   : "v"(xo[0]), "v"(xo[1]), "v"(xo[2]), "s"(rx), "s"(r16) : "memory");
      asm volatile("buffer_load_dwordx2 %0, %7, %12, 0 offen\n\tbuffer_load_dwordx2 %1, %8, %12, 0 offen\n\t"
                   "buffer_load_dwordx2 %2, %9, %12, 0 offen\n\t"
                   "buffer_load_dwordx3 %3, %10, %13, 0 offen sc1\n\tbuffer_load_dwordx3 %4, %10, %13, 0 offen offset:256 sc1\n\t"
                   "buffer_load_ushort %5, %11, %14, 0 offen\n\tbuffer_load_ushort %6, %11, %14, 0 offen offset:32"
                   : "=&v"(dk[0]), "=&v"(dk[1]), "=&v"(dk[2]), "=&v"(sv[0]), "=&v"(sv[1]), "=&v"(bb2[0]), "=&v"(bb2[1])
                   : "v"(co[0]), "v"(co[1]), "v"(co[2]), "v"(so), "v"(bo), "s"(rdk), "s"(rs), "s"(rb2) : "memory");
    };
    auto base_got = [&]() {
      asm volatile("s_waitcnt vmcnt(0)" : "+v"(xx[0][0]), "+v"(xx[0][1]), "+v"(xx[1][0]), "+v"(xx[1][1]), "+v"(xx[2][0]), "+v"(xx[2][1]),
                   "+v"(dk[0]), "+v"(dk[1]), "+v"(dk[2]), "+v"(sv[0]), "+v"(sv[1]), "+v"(bb2[0]), "+v"(bb2[1]) :: "memory");
    };
    if (w < 6) {
      wslot = wl * 8192;
      asm volatile("" : "+s"(wslot));
      WAITV(24);
      BAR();
      w2_any(4);
      w2_any(5);
      _Pragma("unroll 1") for (int k = 6; k < nss; k++) {
        WAITV(32);
        BAR();
        w2_any(k);
      }
      _Pragma("unroll 1") for (int k = nss > 6 ? nss : 6; k < n2; k++) {
        const unsigned so = addr(ord2, k - nss);
        WAITV(32);
        BAR();
        w2_go(so);
      }
      WAITV(32); BAR();
      base_in();
      WAITV(37); BAR();
      WAITV(29); BAR();
      WAITV(21); BAR();
      base_got(); BAR();
      BAR();
    } else {
      _Pragma("unroll 1") for (int k = 0; k < 3; k++) c_go(k);
      WAITV(18);
      BAR(); TR();
      _Pragma("unroll 1") for (int k = 3; k < nss; k++) {
        WAITV(9);
        BAR(); TR();
        c_go(k);
      }
      const unsigned uflag = WS_FU + ((9 * q + ((lane < 18 ? lane : 0) >> 1)) * 5 + j2 + (lane & 1)) * FS;
      WAITV(9); BAR(); TR();
      int ok = 0;
      if (!isA) {
        const v2u a = __builtin_amdgcn_raw_buffer_load_b64(rs, uflag, 0, 16);
        ok = __builtin_amdgcn_readfirstlane(__all(a[0] == tlo && a[1] == thi));
      }
      WAITV(0);
      if (ok) { dmalin<9>(fl, lane * 16, rs, addr(ord2, 0)); fnext(); }
      BAR(); TR();
      if (!ok) {
        if (isA) __builtin_amdgcn_s_sleep(UDELAY);
        poll<1>(rs, uflag, 0, true, tlo, thi);
        dmalin<9>(fl, lane * 16, rs, addr(ord2, 0)); fnext();
      }
      if (w == 6) STAMP(6);
      dmalin<9>(fl, lane * 16, rs, addr(ord2, 1)); fnext();
      _Pragma("unroll 1") for (int k = 2; k < 18; k++) {
        const unsigned so = addr(ord2, k);
        WAITV(9);
        BAR(); TR();
        dmalin<9>(fl, lane * 16, rs, so);
        fnext();
      }
      WAITV(9); BAR(); TR();
      base_in();
      WAITV(13); BAR(); TR();
      BAR(); TR();
      base_got();
    }
    int ln2 = lane;
    asm volatile("" : "+v"(ln2));
    const rsrc_t ro = mk(out, S * D * 4);
    unsigned oo[3], po[3];
#pragma unroll
    for (int u = 0; u < 3; u++) {
      const unsigned off = (u < 2 || lw == 0) ? 0u : 0x80000000u;
      oo[u] = ((st * 128 + 32 * q + (ln2 & 15)) * 2304 + (j2 * 144 + 16 * (lw + 4 * u) + 4 * (ln2 >> 4)) * 4) | off;
      po[u] = (WS_XS + (tile * 4 + q) * 4 * 9216 + (lw + 4 * u) * 1024 + ln2 * 16) | off;
    }
    unsigned fx = WS_FX + ((tile * 4 + q) * 4 + (ln2 & 3)) * FS;
    asm volatile("" : "+v"(po[0]), "+v"(po[1]), "+v"(po[2]), "+v"(fx), "+v"(oo[0]), "+v"(oo[1]), "+v"(oo[2]));
    f32x4 y[3][2];
#pragma unroll
    for (int u = 0; u < 3; u++)
#pragma unroll
      for (int cc = 0; cc < 2; cc++) {
        const float mb = asf(sv[cc][0]), r1 = asf(sv[cc][2]), bv = bflo(bb2[cc]);
#pragma unroll
        for (int h = 0; h < 2; h++) {
          const float x0 = bflo(xx[u][cc][h]), x1 = bfhi(xx[u][cc][h]);
          unsigned t = pk2((x0 - mb) * r1, (x1 - mb) * r1);
          t = pk2(bflo(t) * bflo(dk[u][h]), bfhi(t) * bfhi(dk[u][h]));
          y[u][cc][2 * h] = x0 + bflo(t) + bv;
          y[u][cc][2 * h + 1] = x1 + bfhi(t) + bv;
        }
      }
#pragma unroll
    for (int u = 0; u < 3; u++) asm volatile("" : "+v"(y[u][0]), "+v"(y[u][1]));
    meet(24576, 1);
#pragma unroll
    for (int u = 0; u < 3; u++)
#pragma unroll
      for (int cc = 0; cc < 2; cc++) y[u][cc] += *(LAS f32x4*)(size_t)((lw + 4 * u) * 2048 + cc * 1024 + ln2 * 16);
#pragma unroll
    for (int u = 0; u < 3; u++) asm volatile("" : "+v"(y[u][0]), "+v"(y[u][1]));
    __builtin_amdgcn_s_sleep(POLL_DELAY_X);
    poll<1>(rs, fx, 0, (ln2 & 3) != q, tlo, thi);
    if (w == 4) STAMP(10);
    v4u pv[3][3];
#pragma unroll
    for (int u = 0; u < 3; u++) {
#pragma unroll
      for (int j = 0; j < 3; j++) pv[u][j] = __builtin_amdgcn_raw_buffer_load_b128(rs, po[u], ((q + 1 + j) & 3) * 9216, 16);
    }
    SB();
#pragma unroll
    for (int u = 0; u < 3; u++)
#pragma unroll
      for (int cc = 0; cc < 2; cc++) {
        f32x4 v = y[u][cc];
#pragma unroll
        for (int j = 0; j < 3; j++) {
          v[0] += bflo(pv[u][j][2 * cc]); v[1] += bfhi(pv[u][j][2 * cc]);
          v[2] += bflo(pv[u][j][2 * cc + 1]); v[3] += bfhi(pv[u][j][2 * cc + 1]);
        }
        ST128((__builtin_bit_cast(v4u, v)), ro, oo[u], cc * 16 * 2304, POLO);
      }
    if (b == EPB && w == 4 && lane == 0) put_flag(rs, WS_EPOCH, tlo, thi);
    if (w == 4) STAMP(11);
  }
}

extern "C" void kernel_launch(void* const* d_in, const int* in_sizes, int n_in, void* d_out, int out_size, void* d_ws,
                              size_t ws_size, hipStream_t stream) {
  (void)in_sizes; (void)n_in; (void)out_size; (void)ws_size;
  (void)hipFuncSetAttribute((const void*)fused_kernel, hipFuncAttributeMaxDynamicSharedMemorySize, L_TOTAL);
  fused_kernel<<<256, 512, L_TOTAL, stream>>>(
      (const u16*)d_in[0], (char*)d_ws, (const u16*)d_in[1], (const u16*)d_in[2], (const u16*)d_in[3], (const u16*)d_in[4],
      (const u16*)d_in[5], (const u16*)d_in[6], (const u16*)d_in[7], (const u16*)d_in[8], (const u16*)d_in[10],
      (const u16*)d_in[11], (const u16*)d_in[12], (const u16*)d_in[13], (float*)d_out);
}
```
